```python
import torch

def _softmax(x, axis=-1):
    return torch.softmax(x, dim=axis)
def _gelu(x):
    return torch.nn.functional.gelu(x, approximate="tanh")
def _silu(x):
    return torch.nn.functional.silu(x)
def _sigmoid(x):
    return torch.sigmoid(x)
def _layernorm(x, axis=-1, eps=1e-5):
    m = x.mean(dim=axis, keepdim=True)
    v = x.var(dim=axis, keepdim=True, unbiased=False)
    return (x - m) / torch.sqrt(v + eps)
def _scatter_add(tgt, idx, src, axis=0):
    return tgt.clone().index_add_(axis, idx, src)
def _segment_sum(src, seg, n):
    out = torch.zeros((n,) + src.shape[1:], dtype=src.dtype)
    return out.index_add_(0, seg, src)
def _pad(x, pw):
    flat = tuple(v for pair in reversed(pw) for v in pair)
    return torch.nn.functional.pad(x, flat)

kernel_name = 'concat_heads__sliding_block__softmax_full_mha__prompt'

def setup_inputs():

    _w = (lambda a: (lambda t: t.to(torch.bfloat16) if t.is_floating_point() else t)(torch.as_tensor(a)))
    def _A_setup():
        B, S, D = 1, 2048, 448
        return {
            "x": _w((lambda *s: torch.randn(*s, dtype=torch.bfloat16) * 0.2)(B, S, D)),
            "wqkv": _w((lambda *s: torch.randn(*s, dtype=torch.bfloat16) * 0.8)(D, 3 * D)),
            "wo": _w((lambda *s: torch.randn(*s, dtype=torch.bfloat16) * 3.2)(D, D)),
        }
    _A_in = _A_setup()

    def _B_setup():
        B, S, D = 1, 2048, 448
        return {
            "x": _w((lambda *s: torch.randn(*s, dtype=torch.bfloat16) * 0.2)(B, S, D)),
            "wqkv": _w((lambda *s: torch.randn(*s, dtype=torch.bfloat16) * 0.8)(D, 3 * D)),
            "wo": _w((lambda *s: torch.randn(*s, dtype=torch.bfloat16) * 3.2)(D, D)),
        }
    _B_in = _B_setup()

    return {
        "A_x": _A_in["x"],
        "A_wqkv": _A_in["wqkv"],
        "A_wo": _A_in["wo"],
        "B_x": _B_in["x"],
        "B_wqkv": _B_in["wqkv"],
        "B_wo": _B_in["wo"],
    }

def reference(A_x, A_wqkv, A_wo, B_x, B_wqkv, B_wo):
    def _A_ref(x, wqkv, wo):
        B, S, D = x.shape
        H = 16; Dh = D // H; W = 256
        qkv = (lambda x, *d: x.permute(*d))((x @ wqkv).reshape(B, S, 3, H, Dh), 2, 0, 3, 1, 4)
        q, k, v = qkv[0], qkv[1], qkv[2]
        scores = torch.einsum("bhqd,bhkd->bhqk", q, k) * (Dh ** -0.5)
        idx = torch.arange(S)
        mask = (idx[:, None] - idx[None, :] >= 0) & (idx[:, None] - idx[None, :] < W)
        scores = torch.where(mask[None, None], scores, -1e9)
        p = _softmax(scores, axis=-1)
        o = torch.einsum("bhqk,bhkd->bhqd", p, v)
        return (lambda x, *d: x.permute(*d))(o, 0, 2, 1, 3).reshape(B, S, D) @ wo

    def _B_ref(x, wqkv, wo):
        B, S, D = x.shape
        H = 16; Dh = D // H
        qkv = (lambda x, *d: x.permute(*d))((x @ wqkv).reshape(B, S, 3, H, Dh), 2, 0, 3, 1, 4)
        q, k, v = qkv[0], qkv[1], qkv[2]
        scale = Dh ** -0.5
        scores = torch.einsum("bhqd,bhkd->bhqk", q, k) * scale
        p = _softmax(scores, axis=-1)
        o = torch.einsum("bhqk,bhkd->bhqd", p, v)
        return (lambda x, *d: x.permute(*d))(o, 0, 2, 1, 3).reshape(B, S, D) @ wo

    a = _A_ref(A_x, A_wqkv, A_wo)
    b = _B_ref(B_x, B_wqkv, B_wo)
    return torch.concatenate([a, b], axis=-1)
```

```cpp
#include <hip/hip_runtime.h>
#include <hip/hip_bf16.h>
#include <stdint.h>

typedef float f32x16 __attribute__((ext_vector_type(16)));
typedef float f32x2 __attribute__((ext_vector_type(2)));
typedef __bf16 bf16x8 __attribute__((ext_vector_type(8)));
typedef __bf16 bf16x2 __attribute__((ext_vector_type(2)));
typedef unsigned u32x4 __attribute__((ext_vector_type(4)));
typedef unsigned u32x2 __attribute__((ext_vector_type(2)));
typedef __attribute__((address_space(3))) const char* lds_cptr;
typedef volatile __attribute__((address_space(3))) unsigned* lds_word;
typedef __attribute__((address_space(1))) unsigned long long gu64;
typedef __attribute__((address_space(1))) unsigned gu32;

#define DI __device__ __forceinline__
#define SB() __builtin_amdgcn_sched_barrier(0)
#define PIN(x) asm volatile("" : "+v"(x))
#define MFMA(a, b, c) __builtin_amdgcn_mfma_f32_32x32x16_bf16(__builtin_bit_cast(bf16x8, a), __builtin_bit_cast(bf16x8, b), c, 0, 0, 0)
#define RLX_AGENT __ATOMIC_RELAXED, __HIP_MEMORY_SCOPE_AGENT

constexpr int SEQ = 2048, DM = 448, NH = 16, NQKV = 3 * DM;
constexpr int NT = SEQ / 32;
constexpr float QSCALE = 0.18898223650461362f * 1.4426950408889634f;
constexpr float DELTA = 96.f;
constexpr float NEGBIG = -1.0e30f;
constexpr int SC1 = 16;
constexpr int RSRC3 = 0x00020000;
constexpr unsigned CODE_TOUCH = 32000;

constexpr unsigned WS_Q = 0, WS_Q_BYTES = 2u * NH * NT * 2048u;
constexpr unsigned WS_KV = WS_Q + WS_Q_BYTES, WS_KV_BYTES = 2u * NH * NT * 4096u;
constexpr int FS = 16;
constexpr unsigned WS_FLAG = WS_KV + WS_KV_BYTES;
constexpr unsigned WS_OFLAG = WS_FLAG + 2 * 256 * FS * 8;
constexpr unsigned WS_NONCE = WS_OFLAG + 2 * 256 * FS * 8;

constexpr int P_SLOT = 28672, P_XB = 16384, P_WP = 192, P_NS = 5;
constexpr int LDS_PART = 0;
constexpr int LDS_WO = 49152;
constexpr int LDS_OST = 107520;
constexpr int LDS_CTL = P_NS * P_SLOT, LDS_QX = LDS_CTL + 1024, LDS_BYTES = LDS_QX + 8192;
constexpr int C_XST = LDS_CTL,
              C_AOK = LDS_CTL + 16,
              C_QX = LDS_CTL + 32,
              C_PART = LDS_CTL + 48,
              C_XIS = LDS_CTL + 64,
              C_YST = LDS_CTL + 80,
              C_YBAR = LDS_CTL + 96,
              C_XOS = LDS_CTL + 112,
              C_YOS = LDS_CTL + 128,
              C_OOK = LDS_CTL + 144,
              C_WO = LDS_CTL + 160,
              C_NONCE = LDS_CTL + 176;
constexpr int N_CTL = 11;

DI unsigned rfl(unsigned x) { return __builtin_amdgcn_readfirstlane(x); }
DI unsigned cvtpk(float lo, float hi) { f32x2 v = {lo, hi}; bf16x2 b = __builtin_convertvector(v, bf16x2); return __builtin_bit_cast(unsigned, b); }
DI __amdgpu_buffer_rsrc_t mkrs(const void* p, unsigned bytes) { return __builtin_amdgcn_make_buffer_rsrc((void*)p, (short)0, (int)bytes, RSRC3); }
DI u32x4 bld(__amdgpu_buffer_rsrc_t rs, unsigned voff, unsigned soff) { return __builtin_amdgcn_raw_buffer_load_b128(rs, (int)voff, (int)soff, SC1); }
DI void bst(u32x4 v, __amdgpu_buffer_rsrc_t rs, unsigned voff, unsigned soff) { __builtin_amdgcn_raw_buffer_store_b128(v, rs, (int)(voff + soff), 0, SC1); }
DI void dma16(__amdgpu_buffer_rsrc_t rs, unsigned voff, unsigned soff, unsigned ldsaddr) {
    asm volatile("s_mov_b32 m0, %0\n\ts_nop 0\n\tbuffer_load_dwordx4 %1, %2, %3 offen lds" ::"s"(ldsaddr), "v"(voff), "s"(rs), "s"(soff) : "m0", "memory");
}
#define TOUCH(dst, addr) asm volatile("global_load_dword %0, %1, off" : "=&v"(dst) : "v"(addr) : "memory")
DI u32x2 trread(lds_cptr p) {
    typedef short v4s __attribute__((ext_vector_type(4)));
    return __builtin_bit_cast(u32x2, __builtin_amdgcn_ds_read_tr16_b64_v4i16((__attribute__((address_space(3))) v4s*)p));
}
DI u32x4 ldsread(lds_cptr p) { return *(const __attribute__((address_space(3))) u32x4*)p; }
DI float swapmax(float m) { auto r = __builtin_amdgcn_permlane32_swap(__float_as_uint(m), __float_as_uint(m), false, false); return fmaxf(__uint_as_float(r[0]), __uint_as_float(r[1])); }
DI float max16(const f32x16& s) {
    float a = fmaxf(fmaxf(s[0], s[1]), s[2]), b = fmaxf(fmaxf(s[3], s[4]), s[5]);
    a = fmaxf(fmaxf(a, s[6]), s[7]); b = fmaxf(fmaxf(b, s[8]), s[9]); a = fmaxf(fmaxf(a, s[10]), s[11]); b = fmaxf(fmaxf(b, s[12]), s[13]);
    a = fmaxf(fmaxf(a, s[14]), s[15]); return fmaxf(a, b);
}
DI int crow(int i, int h) { return (i & 3) + 8 * (i >> 2) + 4 * h; }
DI float ex2(float x) { return __builtin_amdgcn_exp2f(x); }

DI unsigned long long mktag(unsigned nonce) { return (unsigned long long)nonce | ((unsigned long long)(~nonce ^ 0x5bd1e995u) << 32); }
DI void publish(char* ws, unsigned line, unsigned long long tag) {
    if ((threadIdx.x & 63) < 8) {
        const u32x4 v = {(unsigned)tag, (unsigned)(tag >> 32), (unsigned)tag, (unsigned)(tag >> 32)};
        bst(v, mkrs(ws + WS_FLAG, 4 * 256 * FS * 8), (threadIdx.x & 63) * 16, line * (FS * 8));
    }
}
template <int SLEEP> DI void wait_flags(const gu64* flags, unsigned idx, bool need, unsigned long long tag) {
#pragma unroll 1
    for (unsigned spins = 0; spins < (1u << 17); ++spins) {
        const unsigned long long x = __hip_atomic_load(flags + idx * FS, RLX_AGENT);
        if (__all(!need || x == tag)) break;
        __builtin_amdgcn_s_sleep(SLEEP);
    }
    asm volatile("" ::: "memory");
}
DI unsigned lds_add(char* lds, int off) {
    unsigned old = 0;
    if ((threadIdx.x & 63) == 0) old = __hip_atomic_fetch_add((__attribute__((address_space(3))) unsigned*)(lds + off), 1u, __ATOMIC_RELAXED, __HIP_MEMORY_SCOPE_WORKGROUP);
    return rfl(old);
}
template <int SLEEP> DI void lds_wait(char* lds, int off, unsigned n) {
    lds_word p = (lds_word)(lds + off);
#pragma unroll 1
    for (unsigned spins = 0; spins < (1u << 20) && *p < n; ++spins) __builtin_amdgcn_s_sleep(SLEEP);
    asm volatile("" ::: "memory");
}

DI f32x16 exact_tile(__amdgpu_buffer_rsrc_t rskv, unsigned kvbase, u32x4 q0, u32x4 q1, int t0, int t1, int tq, bool causal, int lane) {
    const int r32 = lane & 31, h = lane >> 5;
    if (h) q1[2] = 0;
    float m = NEGBIG;
    for (int t = t0; t <= t1; ++t) {
        const u32x4 k0 = bld(rskv, lane * 16, kvbase + t * 4096), k1 = bld(rskv, lane * 16, kvbase + t * 4096 + 1024);
        f32x16 s = {}; s = MFMA(k0, q0, s); s = MFMA(k1, q1, s);
        if (causal) {
#pragma unroll
            for (int i = 0; i < 16; ++i) { const int d = (tq - t) * 32 + r32 - crow(i, h); if (d < 0 || d >= 256) s[i] = NEGBIG; }
        }
        m = fmaxf(m, max16(s));
    }
    m = swapmax(m);
    f32x16 o = {};
    for (int t = t0; t <= t1; ++t) {
        const u32x4 k0 = bld(rskv, lane * 16, kvbase + t * 4096), k1 = bld(rskv, lane * 16, kvbase + t * 4096 + 1024);
        const u32x4 v0 = bld(rskv, lane * 16, kvbase + t * 4096 + 2048), v1 = bld(rskv, lane * 16, kvbase + t * 4096 + 3072);
        f32x16 s = {}; s = MFMA(k0, q0, s); s = MFMA(k1, q1, s);
        u32x4 pa, pb;
#pragma unroll
        for (int i = 0; i < 16; ++i) {
            s[i] = ex2(s[i] - m);
            if (causal) { const int d = (tq - t) * 32 + r32 - crow(i, h); if (d < 0 || d >= 256) s[i] = 0.f; }
        }
#pragma unroll
        for (int i = 0; i < 4; ++i) { pa[i] = cvtpk(s[2 * i], s[2 * i + 1]); pb[i] = cvtpk(s[8 + 2 * i], s[8 + 2 * i + 1]); }
        o = MFMA(v0, pa, o); o = MFMA(v1, pb, o);
    }
    return o;
}
DI float rowsum(const f32x16& o) { auto r = __builtin_amdgcn_permlane32_swap(__float_as_uint(o[12]), __float_as_uint(o[12]), false, false); return __uint_as_float(r[1]); }
DI bool unsafe(const f32x16& o, float l) {
    float z = 0.f;
#pragma unroll
    for (int i = 0; i < 16; ++i) z = __builtin_fmaf(o[i], 0.f, z);
    const bool ok = (z == 0.f) && (l > 0x1p-106f) && (l < 0x1p100f);
    return __any(!ok);
}
DI void store_o(const f32x16& o, float l, __amdgpu_buffer_rsrc_t rso, unsigned off, int lane) {
    const float rl = __builtin_amdgcn_rcpf(l); u32x4 a, c2;
#pragma unroll
    for (int i = 0; i < 4; ++i) { a[i] = cvtpk(o[2 * i] * rl, o[2 * i + 1] * rl); c2[i] = cvtpk(o[8 + 2 * i] * rl, o[8 + 2 * i + 1] * rl); }
    if (lane >> 5) { c2[2] = 0; c2[3] = 0; }
    bst(a, rso, lane * 16, off); bst(c2, rso, lane * 16, off + 1024);
}
#define EXPCVT(S, b4, P, i) do { const float e0_ = ex2(S[b4]), e1_ = ex2(S[b4 + 1]), e2_ = ex2(S[b4 + 2]), e3_ = ex2(S[b4 + 3]); \
        P[i] = cvtpk(e0_, e1_); P[i + 1] = cvtpk(e2_, e3_); PIN(P); } while (0)

struct Proj {
    unsigned ax[4], aw[3];
    f32x16 acc[3];
    u32x4 xf[4], wf[4][3];
    u32x4 id0, id1;
};
DI void p_rdx(Proj& p, lds_cptr L, int buf, int slot, int ks) { p.xf[buf] = ldsread(L + slot * P_SLOT + p.ax[ks]); }
DI void p_rdw(Proj& p, lds_cptr L, int buf, int slot, int ks, int c) {
    lds_cptr wp = L + slot * P_SLOT + p.aw[c] + ks * (16 * P_WP);
    const u32x2 a = trread(wp), b2 = trread(wp + 4 * P_WP);
    p.wf[buf][c] = (u32x4){a[0], a[1], b2[0], b2[1]};
}
template <class Side>
DI void p_kstep(Proj& p, lds_cptr L, int buf, bool first, int nb, bool rd, int slot, int ks2, Side side) {
    const f32x16 zero = {};
    p.acc[0] = MFMA(p.wf[buf][0], p.xf[buf], first ? zero : p.acc[0]); SB(); if (rd) { p_rdx(p, L, nb, slot, ks2); p_rdw(p, L, nb, slot, ks2, 0); } side(0); SB();
    p.acc[1] = MFMA(p.wf[buf][1], p.xf[buf], first ? zero : p.acc[1]); SB(); if (rd) { p_rdw(p, L, nb, slot, ks2, 1); } side(1); SB();
    p.acc[2] = MFMA(p.wf[buf][2], p.xf[buf], first ? zero : p.acc[2]); SB(); if (rd) { p_rdw(p, L, nb, slot, ks2, 2); } side(2); SB();
}
DI void p_setup(Proj& p, int w, int lane, int cb0) {
    const int r32 = lane & 31, h = lane >> 5;
#pragma unroll
    for (int ks = 0; ks < 4; ++ks) p.ax[ks] = (32 * w + r32) * 128 + ((((2 * ks) | h) ^ ((r32 >> 1) & 7)) << 4);
    const int nh = (lane >> 4) & 1, q = (lane & 15) >> 2, pq = lane & 3, pqs = ((pq & 1) << 1) | (pq >> 1);
#pragma unroll
    for (int c = 0; c < 3; ++c) p.aw[c] = P_XB + (8 * h + q) * P_WP + (cb0 & 8) + 56 * c + 32 * nh + 8 * pqs;
#pragma unroll
    for (int e = 0; e < 4; ++e) {
        const int d0 = 8 * h + 2 * e;
        p.id0[e] = (r32 == d0 ? 0x3F80u : 0u) | (r32 == d0 + 1 ? 0x3F800000u : 0u);
        p.id1[e] = (r32 == 16 + d0 ? 0x3F80u : 0u) | (r32 == 17 + d0 ? 0x3F800000u : 0u);
    }
    PIN(p.id0); PIN(p.id1);
#pragma unroll
    for (int i = 0; i < 4; ++i) PIN(p.ax[i]);
#pragma unroll
    for (int i = 0; i < 3; ++i) PIN(p.aw[i]);
}
template <int S0, class Sync, class Piece>
DI void p_run(Proj& p, lds_cptr L, Sync sync, Piece piece) {
    constexpr int sl0 = S0 % P_NS;
    p_rdx(p, L, 0, sl0, 0); p_rdw(p, L, 0, sl0, 0, 0); p_rdw(p, L, 0, sl0, 0, 1); p_rdw(p, L, 0, sl0, 0, 2);
    p_rdx(p, L, 1, sl0, 1); p_rdw(p, L, 1, sl0, 1, 0); p_rdw(p, L, 1, sl0, 1, 1); p_rdw(p, L, 1, sl0, 1, 2); SB();
#pragma unroll
    for (int sb = 0; sb < 7; ++sb) {
        const int s = S0 + sb, sl = s % P_NS, sn = (s + 1) % P_NS;
        p_kstep(p, L, 0, sb == 0, 2, true, sl, 2, [&](int i) { if (sb > 0 && i < 2) piece(s - 1, 5 + i); });
        p_kstep(p, L, 1, false, 3, true, sl, 3, [&](int) {});
        p.acc[0] = MFMA(p.wf[2][0], p.xf[2], p.acc[0]); SB();
        sync(s);
        if (sb < 6) { p_rdx(p, L, 0, sn, 0); p_rdw(p, L, 0, sn, 0, 0); } SB();
        p.acc[1] = MFMA(p.wf[2][1], p.xf[2], p.acc[1]); SB(); if (sb < 6) p_rdw(p, L, 0, sn, 0, 1); piece(s, 0); SB();
        p.acc[2] = MFMA(p.wf[2][2], p.xf[2], p.acc[2]); SB(); if (sb < 6) p_rdw(p, L, 0, sn, 0, 2); piece(s, 1); SB();
        p_kstep(p, L, 3, false, 1, sb < 6, sn, 1, [&](int i) { piece(s, 2 + i); });
    }
}
DI void p_store(Proj& p, char* ws, int b, int jg, int tile, int lane) {
    const int h = lane >> 5;
    const f32x16 zero = {};
    const __amdgpu_buffer_rsrc_t rsall = mkrs(ws, WS_Q_BYTES + WS_KV_BYTES);
    u32x4 f[3][2];
#pragma unroll
    for (int c = 0; c < 3; ++c) {
        const int part = (3 * jg + c) >> 4;
        const f32x16 v = p.acc[c] * (part == 0 ? QSCALE : 1.0f);
#pragma unroll
        for (int i = 0; i < 4; ++i) { f[c][0][i] = cvtpk(v[2 * i], v[2 * i + 1]); f[c][1][i] = cvtpk(v[8 + 2 * i], v[8 + 2 * i + 1]); }
        if (h) { f[c][1][2] = part == 0 ? 0u : 0x00003F80u; f[c][1][3] = 0; }
        if (part == 2) { p.acc[c] = MFMA(f[c][0], p.id0, zero); p.acc[c] = MFMA(f[c][1], p.id1, p.acc[c]); }
        else {
            const unsigned th = (b * NH + ((3 * jg + c) & 15)) * NT + tile, o = part == 0 ? WS_Q + th * 2048u : WS_KV + th * 4096u;
            bst(f[c][0], rsall, lane * 16, o); bst(f[c][1], rsall, lane * 16 + 1024, o);
        }
    }
#pragma unroll
    for (int c = 0; c < 3; ++c) {
        if (((3 * jg + c) >> 4) == 2) {
#pragma unroll
            for (int i = 0; i < 4; ++i) { f[c][0][i] = cvtpk(p.acc[c][2 * i], p.acc[c][2 * i + 1]); f[c][1][i] = cvtpk(p.acc[c][8 + 2 * i], p.acc[c][8 + 2 * i + 1]); }
            const unsigned o = WS_KV + ((b * NH + ((3 * jg + c) & 15)) * NT + tile) * 4096u + 2048u;
            bst(f[c][0], rsall, lane * 16, o); bst(f[c][1], rsall, lane * 16 + 1024, o);
        }
    }
}

DI void out_tile(char* lds, void* out, __amdgpu_buffer_rsrc_t rso, int ob, int otb, int og, int w, int lane) {
    const int r32 = lane & 31, h = lane >> 5;
    const unsigned lo = lane * 16;
    lds_cptr L = (lds_cptr)lds;
    const f32x16 zero = {};
    const int tile = otb * 4 + w;
    const unsigned obase = (ob * NH * NT + tile) * 2048u;
    u32x4 of[32];
#pragma unroll
    for (int i = 0; i < 32; ++i) of[i] = bld(rso, lo + (i & 1) * 1024, obase + (i >> 1) * (NT * 2048));
    unsigned aw;
    {
        const int nh = (lane >> 4) & 1, q = (lane & 15) >> 2, pq = lane & 3, pqs = ((pq & 1) << 1) | (pq >> 1);
        const int cb = 32 * nh + 8 * pqs;
        aw = LDS_WO + (4 * h + q) * 128 + (((cb >> 4) ^ (4 * (q >> 1))) << 4) + (cb & 8);
    }
    lds_wait<0>(lds, C_WO, 4);
    f32x16 acc[2];
    u32x4 wf[2][2];
    auto rd = [&](int buf, int ks, int fb) {
        const int hd = ks >> 1, s2 = ks & 1;
        lds_cptr wp = L + ((aw ^ (fb * 64)) + (28 * hd + 16 * s2) * 128);
        const u32x2 x = trread(wp), y = trread(wp + 8 * 128);
        wf[buf][fb] = (u32x4){x[0], x[1], y[0], y[1]};
    };
    rd(0, 0, 0); rd(0, 0, 1); SB();
#pragma unroll
    for (int ks = 0; ks < 32; ++ks) {
        acc[0] = MFMA(wf[ks & 1][0], of[ks], ks == 0 ? zero : acc[0]); SB(); if (ks + 1 < 32) rd((ks + 1) & 1, ks + 1, 0); SB();
        acc[1] = MFMA(wf[ks & 1][1], of[ks], ks == 0 ? zero : acc[1]); SB(); if (ks + 1 < 32) rd((ks + 1) & 1, ks + 1, 1); SB();
    }
    const __amdgpu_buffer_rsrc_t rsout = mkrs(out, SEQ * 2 * DM * 2);
    char* stg = lds + LDS_OST + w * 4096;
#pragma unroll
    for (int fb = 0; fb < 2; ++fb) {
        u32x4 x, y;
#pragma unroll
        for (int i = 0; i < 4; ++i) { x[i] = cvtpk(acc[fb][2 * i], acc[fb][2 * i + 1]); y[i] = cvtpk(acc[fb][8 + 2 * i], acc[fb][8 + 2 * i + 1]); }
        *(u32x4*)(stg + r32 * 128 + (((fb * 4 + h) ^ (r32 & 7)) << 4)) = x;
        *(u32x4*)(stg + r32 * 128 + (((fb * 4 + 2 + h) ^ (r32 & 7)) << 4)) = y;
    }
    const int a = lane >> 3, pc = lane & 7;
#pragma unroll
    for (int k = 0; k < 4; ++k) {
        const u32x4 v = *(const u32x4*)(stg + (8 * k + a) * 128 + ((pc ^ a) << 4));
        bst(v, rsout, (tile * 32 + 8 * k + a) * (2 * DM * 2) + pc * 16, ob * (DM * 2) + og * 128);
    }
}

__global__ __launch_bounds__(512, 2) void k_main(const void* __restrict__ xA, const void* __restrict__ wA, const void* __restrict__ woA, const void* __restrict__ xB,
                                                 const void* __restrict__ wB, const void* __restrict__ woB, char* __restrict__ ws, void* __restrict__ out) {
    unsigned long long kpc; asm volatile("s_getpc_b64 %0" : "=s"(kpc)); kpc &= ~255ull;
    extern __shared__ __attribute__((aligned(16))) char lds[];
    const int tid = threadIdx.x, lane = tid & 63, wv = rfl(tid >> 6), w = wv & 3;
    const int id = blockIdx.x;
    const int pc_ = id & 7, pr = id >> 3;
    const int tb = (pc_ & 3) * 4 + (pr & 3);
    const int jg = (int)(((pc_ < 4 ? 0xBA54C963u : 0xFED87210u) >> (4 * (pr >> 2))) & 15);
    const int head = 2 * (id & 7) + (id >> 7), j = (id >> 3) & 15;
    const int tq = 4 * j + w;
    const int ob_ = pc_ >> 2, otb = (pc_ & 3) * 4 + (pr & 3), og = pr >> 2;

    gu64* flags = (gu64*)(ws + WS_FLAG);
    gu64* oflags = (gu64*)(ws + WS_OFLAG);
    const unsigned lds0 = (unsigned)(uintptr_t)lds;
    const int r32 = lane & 31, h = lane >> 5;
    const unsigned lo = lane * 16;
    const __amdgpu_buffer_rsrc_t rsq = mkrs(ws + WS_Q, WS_Q_BYTES), rskv = mkrs(ws + WS_KV, WS_KV_BYTES);
    const int cb0 = 168 * jg;
    const int gk = (16 + head) / 3, gv = (32 + head) / 3, gq = head / 3;
    const f32x16 zero = {};
    lds_cptr L = (lds_cptr)lds;

    const __amdgpu_buffer_rsrc_t rsxB = mkrs(xB, SEQ * DM * 2), rswB = mkrs(wB, DM * NQKV * 2), rsxA = mkrs(xA, SEQ * DM * 2), rswA = mkrs(wA, DM * NQKV * 2);
    unsigned vx[4] = {}, vw[3] = {};
    auto stage = [&](int s) {
        const unsigned slot = lds0 + (s % P_NS) * P_SLOT;
        const int sb = s >= 7 ? s - 7 : s;
        const __amdgpu_buffer_rsrc_t rx = s >= 7 ? rsxA : rsxB, rw = s >= 7 ? rswA : rswB;
#pragma unroll
        for (int i = 0; i < 4; ++i) dma16(rx, vx[i], rfl(sb * 128), rfl(slot + (4 * w + i) * 1024));
#pragma unroll
        for (int i = 0; i < 3; ++i) dma16(rw, vw[i], rfl(sb * 64 * NQKV * 2), rfl(slot + P_XB + (3 * w + i) * 1024));
    };
    if (wv >= 4) {
        const int a = lane >> 3, pc = lane & 7;
#pragma unroll
        for (int i = 0; i < 4; ++i) { const int row = tb * 128 + 32 * w + 8 * i + a; vx[i] = row * (DM * 2) + ((pc ^ (((a >> 1) | (4 * (i & 1))))) << 4); }
#pragma unroll
        for (int i = 0; i < 3; ++i) {
            const int u = 64 * (3 * w + i) + lane, row = u / 12, pp = u - 12 * row;
            vw[i] = row * (NQKV * 2) + (cb0 & ~15) + (pp > 10 ? 10 : pp) * 16;
        }
        stage(0); stage(1);
    }
    int role = wv; asm volatile("" : "+s"(role));

    if (role >= 4) {
        Proj p; p_setup(p, w, lane, cb0);
        auto piece1 = [&](int s, int n) {
            const unsigned slot = lds0 + (s % P_NS) * P_SLOT;
            const int sb = s - 7;
            if (n < 4) dma16(rsxA, vx[n], rfl(sb * 128), rfl(slot + (4 * w + n) * 1024));
            else dma16(rswA, vw[n - 4], rfl(sb * 64 * NQKV * 2), rfl(slot + P_XB + (3 * w + n - 4) * 1024));
        };
#define L_WAIT(n) asm volatile("s_waitcnt vmcnt(" #n ")\n\ts_barrier" ::: "memory")
        L_WAIT(7);
        stage(2); L_WAIT(7);
        stage(3); L_WAIT(7);
        stage(4); L_WAIT(7);
        stage(5); L_WAIT(7);
        stage(6); L_WAIT(7);
        stage(7); L_WAIT(7);
        stage(8); stage(9);
        __builtin_amdgcn_s_setprio(1);
        unsigned ybar = 0;
        auto ymeet = [&]() { asm volatile("s_waitcnt lgkmcnt(0)" ::: "memory"); lds_add(lds, C_YBAR); ybar += 4; lds_wait<0>(lds, C_YBAR, ybar); };
        lds_wait<1>(lds, C_XIS, 4);
        stage(10); stage(11);
        asm volatile("s_waitcnt vmcnt(14)" ::: "memory"); ymeet();
        p_run<7>(p, L, [&](int s) {
            if (s == 8 || s == 11) { asm volatile("s_waitcnt vmcnt(0)" ::: "memory"); ymeet(); }
        }, [&](int s, int n) {
            if (s == 8) { piece1(12 + (2 * n) / 7, (2 * n) % 7); piece1(12 + (2 * n + 1) / 7, (2 * n + 1) % 7); }
        });
        p_store(p, ws, 0, jg, tb * 4 + w, lane);
        const unsigned long long tag = mktag(*(lds_word)(lds + C_NONCE) + 1u);
        asm volatile("s_waitcnt vmcnt(0)" ::: "memory");
        if (lds_add(lds, C_YST) == 3) publish(ws, jg * 16 + tb, tag);
        if (pr < 28) {
            lds_wait<1>(lds, C_YST, 4);
            const __amdgpu_buffer_rsrc_t rsw = mkrs(ob_ ? woB : woA, DM * DM * 2);
            const int a = lane >> 3, pc = lane & 7;
            const unsigned v = a * (DM * 2) + og * 128 + ((pc ^ (4 * ((a >> 1) & 1))) << 4);
#pragma unroll 1
            for (int i = w; i < 57; i += 4) dma16(rsw, v, rfl((i < 56 ? i : 55) * 8 * DM * 2), rfl(lds0 + LDS_WO + i * 1024));
            asm volatile("s_waitcnt vmcnt(0)" ::: "memory");
            lds_add(lds, C_WO);
        }
        if (w == 0) {
            const int a3 = lane % 3, blk = j - a3;
            const unsigned idx = lane < 3 ? gk * 16 + blk : lane < 6 ? gv * 16 + blk : gq * 16 + j;
            wait_flags<8>(flags, (blk < 0 && lane < 6) ? 0 : idx, lane <= 6 && !(blk < 0 && lane < 6), tag);
            if (lane == 0) *(lds_word)(lds + C_AOK) = 1;
        } else {
            lds_wait<8>(lds, C_AOK, 1);
        }
        __builtin_amdgcn_s_setprio(0);
        const unsigned kvA = head * NT * 4096u, qoA = (head * NT + tq) * 2048u;
        const u32x4 qa0 = bld(rsq, lo, qoA); u32x4 qa1 = bld(rsq, lo + 1024, qoA);
        f32x16 oa = {};
        {
            u32x4 kb[9][2], vb[9][2];
#pragma unroll
            for (int u = 0; u < 9; ++u) {
                const unsigned so = (tq - 8 + u >= 0) ? kvA + (tq - 8 + u) * 4096u : WS_KV_BYTES;
                kb[u][0] = bld(rskv, lo, so); kb[u][1] = bld(rskv, lo + 1024, so); vb[u][0] = bld(rskv, lo + 2048, so); vb[u][1] = bld(rskv, lo + 3072, so);
            }
            f32x16 cd, cm;
#pragma unroll
            for (int i = 0; i < 16; ++i) { cd[i] = (crow(i, h) > r32) ? NEGBIG : 0.f; cm[i] = __uint_as_float(__float_as_uint(cd[i]) ^ __float_as_uint(NEGBIG)); }
            {
                f32x16 s = MFMA(kb[8][0], qa0, cd); s = MFMA(kb[8][1], qa1, s);
                const float m = swapmax(max16(s));
                if (h) qa1[2] = cvtpk(-(m + DELTA), 0.f);
            }
            f32x16 sA, sB; u32x4 pa, pb;
            sA = MFMA(kb[0][0], qa0, cm); sA = MFMA(kb[0][1], qa1, sA);
#define AUNIT(SC, SN, U)                                                                                                  \
            { if ((U) < 8) { SN = MFMA(kb[((U) + 1) % 9][0], qa0, ((U) == 7 ? cd : zero)); } SB();                         \
              EXPCVT(SC, 0, pa, 0); SB();                                                                                 \
              if ((U) < 8) { SN = MFMA(kb[((U) + 1) % 9][1], qa1, SN); } SB();                                             \
              EXPCVT(SC, 4, pa, 2); SB();                                                                                 \
              if ((U) > 0) { oa = MFMA(vb[((U) + 8) % 9][1], pb, oa); } SB();                                              \
              EXPCVT(SC, 8, pb, 0); SB();                                                                                 \
              oa = MFMA(vb[U][0], pa, oa); SB();                                                                          \
              EXPCVT(SC, 12, pb, 2); SB(); }
            __builtin_amdgcn_s_setprio(1);
            AUNIT(sA, sB, 0) AUNIT(sB, sA, 1)
            __builtin_amdgcn_s_setprio(0);
            AUNIT(sA, sB, 2) AUNIT(sB, sA, 3) AUNIT(sA, sB, 4) AUNIT(sB, sA, 5) AUNIT(sA, sB, 6) AUNIT(sB, sA, 7) AUNIT(sA, sB, 8)
            oa = MFMA(vb[8][1], pb, oa);
        }
        float la = rowsum(oa);
        if (unsafe(oa, la)) { oa = exact_tile(rskv, kvA, qa0, qa1, max(tq - 8, 0), tq, tq, true, lane); la = rowsum(oa); }
        store_o(oa, la, rsq, qoA, lane);
        asm volatile("s_waitcnt vmcnt(0)" ::: "memory");
        if (lds_add(lds, C_YOS) == 3) publish(ws, 512 + j * 16 + head, tag);
        if (pr >= 28 || ob_ != 0) __builtin_amdgcn_endpgm();
        if (w == 0) {
            wait_flags<4>(oflags + otb * 16 * FS, lane & 15, true, tag);
            if (lane == 0) *(lds_word)(lds + C_OOK) = 1;
        } else lds_wait<4>(lds, C_OOK, 1);
        out_tile(lds, out, rsq, 0, otb, og, w, lane);
        __builtin_amdgcn_endpgm();
    }

    if (tid < N_CTL) *(lds_word)(lds + LDS_CTL + 16 * tid) = 0;
    unsigned t_in0, t_in1, t_misc, t_code, nonce;
    {
        const int gw = id * 4 + w;
        if (gw >= 371) __builtin_amdgcn_s_sleep(24);
        const char* base; int c = gw < 840 ? gw : 0;
        if (c < 224) base = (const char*)xB; else if ((c -= 224) < 147) base = (const char*)wB; else if ((c -= 147) < 224) base = (const char*)xA;
        else if ((c -= 224) < 147) base = (const char*)wA; else if ((c -= 147) < 49) base = (const char*)woA; else { c -= 49; base = (const char*)woB; }
        const char* a0 = base + c * 8192 + lane * 64; const char* a1 = a0 + 4096;
        TOUCH(t_in0, a0); TOUCH(t_in1, a1);
        const int l4 = lane < 4 ? lane : 0, l42 = lane < 42 ? lane : 0;
        const char* a2 = w == 1 ? ws + WS_FLAG + (id * 4 + l4) * (FS * 8)
                       : w != 2 ? (const char*)xB
                       : l42 < 19 ? (const char*)wB + l42 * 65536 : l42 < 38 ? (const char*)wA + (l42 - 19) * 65536
                       : (l42 < 40 ? (const char*)xB : (const char*)xA) + tb * (128 * DM * 2) + (l42 & 1) * 65536;
        TOUCH(t_misc, a2);
        const char* a3 = ws + WS_NONCE;
        asm volatile("global_load_dword %0, %1, off sc1" : "=&v"(nonce) : "v"(a3) : "memory");
        const unsigned off = (pr * 4 + w) * 256 + (lane & 3) * 64;
        const char* a4 = (const char*)kpc + (off < CODE_TOUCH ? off : 0);
        TOUCH(t_code, a4);
    }
    {
        Proj p; p_setup(p, w, lane, cb0);
#define P_BAR() asm volatile("s_waitcnt lgkmcnt(0)\n\ts_barrier" ::: "memory")
        P_BAR();
        p_run<0>(p, L, [&](int s) {
            if (s == 5) {
                asm volatile("s_waitcnt vmcnt(0)" : "+v"(t_in0), "+v"(t_in1), "+v"(t_misc), "+v"(t_code), "+v"(nonce) :: "memory");
                if (tid == 0) *(lds_word)(lds + C_NONCE) = nonce;
            }
            if (s < 6) P_BAR();
        }, [&](int, int) {});
        p_store(p, ws, 1, jg, tb * 4 + w, lane);
    }
    lds_add(lds, C_XIS);
    nonce += 1u;
    const unsigned long long tag = mktag(nonce);
    asm volatile("s_waitcnt vmcnt(0)" ::: "memory");
    if (lds_add(lds, C_XST) == 3) publish(ws, 256 + jg * 16 + tb, tag);

    const unsigned kvB = (NH + head) * NT * 4096u, kvw = kvB + 16 * w * 4096u;
    u32x4 q[4][2], kb[4][2], vb[4][2];
    const gu64* fB = flags + 256 * FS + (lane < 8 ? (lane < 4 ? gk : gv) * 16 + 4 * w + (lane & 3) : lane == 8 ? gq * 16 + j : gk * 16 + j) * FS;
    unsigned long long seen = 0;
#pragma unroll 1
    for (unsigned spins = 0; spins < (1u << 17); ++spins) {
        seen = __ballot(__hip_atomic_load(fB, RLX_AGENT) == tag);
        if ((seen & 0x300) == 0x300) break;
        __builtin_amdgcn_s_sleep(4);
    }
    asm volatile("" ::: "memory");
    const unsigned qoB = ((NH + head) * NT + tq) * 2048u;
    u32x4 q0 = bld(rsq, lo, qoB), q1 = bld(rsq, lo + 1024, qoB);
    const u32x4 kd0 = bld(rskv, lo, kvB + tq * 4096u), kd1 = bld(rskv, lo + 1024, kvB + tq * 4096u);
    SB();
#pragma unroll 1
    for (unsigned spins = 0; spins < (1u << 17) && (seen & 0x11) != 0x11; ++spins) {
        seen = __ballot(__hip_atomic_load(fB, RLX_AGENT) == tag);
        if ((seen & 0x11) != 0x11) __builtin_amdgcn_s_sleep(4);
    }
    asm volatile("" ::: "memory");
    kb[0][0] = bld(rskv, lo, kvw); kb[0][1] = bld(rskv, lo + 1024, kvw); vb[0][0] = bld(rskv, lo + 2048, kvw); vb[0][1] = bld(rskv, lo + 3072, kvw);
    unsigned long long x2 = tag;
    if ((seen & 0xFF) != 0xFF) x2 = __hip_atomic_load(fB, RLX_AGENT);
    SB();
    {
        f32x16 s = MFMA(kd0, q0, zero); s = MFMA(kd1, q1, s);
        const float m = swapmax(max16(s));
        if (h) q1[2] = cvtpk(-(m + DELTA), 0.f);
    }
    *(u32x4*)(lds + LDS_QX + w * 2048 + lo) = q0; *(u32x4*)(lds + LDS_QX + w * 2048 + 1024 + lo) = q1;
    asm volatile("s_waitcnt lgkmcnt(0)" ::: "memory");
    lds_add(lds, C_QX);
#pragma unroll
    for (int t = 1; t < 3; ++t) { kb[t][0] = bld(rskv, lo, kvw + t * 4096); kb[t][1] = bld(rskv, lo + 1024, kvw + t * 4096); vb[t][0] = bld(rskv, lo + 2048, kvw + t * 4096); vb[t][1] = bld(rskv, lo + 3072, kvw + t * 4096); }
    SB();
    lds_wait<0>(lds, C_QX, 4);
#pragma unroll
    for (int i = 0; i < 4; ++i) { const int qb = (w + 1 + i) & 3;
        q[i][0] = *(const u32x4*)(lds + LDS_QX + qb * 2048 + lo); q[i][1] = *(const u32x4*)(lds + LDS_QX + qb * 2048 + 1024 + lo); }
    if ((__ballot(x2 == tag) & 0xFF) != 0xFF) wait_flags<4>(fB, 0, lane < 8, tag);

    vb[3][1] = (u32x4){0, 0, 0, 0};
    f32x16 o[4] = {}; f32x16 sA, sB;
    u32x4 pa, pb = {0, 0, 0, 0};
#pragma unroll
    for (int i = 0; i < 4; ++i) { PIN(q[i][0]); PIN(q[i][1]); }
    sA = MFMA(kb[0][0], q[0][0], zero); sA = MFMA(kb[0][1], q[0][1], sA);
#define UNIT(SC, SN, T, B, LD)                                                                                           \
    { constexpr int Tn = ((B) == 3) ? (((T) + 1) & 3) : (T), Bn = ((B) + 1) & 3, Tp = ((B) == 0) ? (((T) + 3) & 3) : (T), Bp = ((B) + 3) & 3; \
      if ((B) == 3) { asm volatile("" :: "v"(kb[Tn][0]), "v"(kb[Tn][1])); SB(); }        \
      SN = MFMA(kb[Tn][0], q[Bn][0], zero); SB(); EXPCVT(SC, 0, pa, 0); SB();                                         \
      SN = MFMA(kb[Tn][1], q[Bn][1], SN); SB(); EXPCVT(SC, 4, pa, 2); SB();                                           \
      o[Bp] = MFMA(vb[Tp][1], pb, o[Bp]); SB(); LD; SB(); EXPCVT(SC, 8, pb, 0); SB();                                 \
      if ((B) == 0) { asm volatile("" :: "v"(vb[T][0]), "v"(vb[T][1])); SB(); }                \
      o[B] = MFMA(vb[T][0], pa, o[B]); SB(); EXPCVT(SC, 12, pb, 2); SB(); }
#define TILE(T, so)                                                                                                      \
    UNIT(sA, sB, T, 0, (kb[((T) + 3) & 3][0] = bld(rskv, lo, so)))                                                    \
    UNIT(sB, sA, T, 1, (kb[((T) + 3) & 3][1] = bld(rskv, lo + 1024, so)))                                             \
    UNIT(sA, sB, T, 2, (vb[((T) + 3) & 3][0] = bld(rskv, lo + 2048, so)))                                             \
    UNIT(sB, sA, T, 3, (vb[((T) + 3) & 3][1] = bld(rskv, lo + 3072, so)))
    auto put = [&](int i) {
        char* dst = lds + LDS_PART + (((w + 1 + i) & 3) * 3 + i) * 4096 + lo;
#pragma unroll
        for (int g = 0; g < 4; ++g) *(float4*)(dst + g * 1024) = make_float4(o[i][4 * g], o[i][4 * g + 1], o[i][4 * g + 2], o[i][4 * g + 3]);
    };
    unsigned sn = kvw + 3 * 4096u;
#pragma unroll 1
    for (int t = 0; t < 16; t += 4) {
        const unsigned sl = kvw + 15 * 4096u;
        const unsigned s0 = sn, s1 = min(sn + 4096u, sl), s2 = min(sn + 8192u, sl), s3 = min(sn + 12288u, sl); sn += 16384u;
        if (t == 12) lds_wait<2>(lds, C_YST, 4);
        TILE(0, s0) TILE(1, s1) TILE(2, s2)
        UNIT(sA, sB, 3, 0, (kb[2][0] = bld(rskv, lo, s3)))
        UNIT(sB, sA, 3, 1, (kb[2][1] = bld(rskv, lo + 1024, s3)))
        UNIT(sA, sB, 3, 2, (vb[2][0] = bld(rskv, lo + 2048, s3)))
        if (t == 12) put(0);
        SB();
        UNIT(sB, sA, 3, 3, (vb[2][1] = bld(rskv, lo + 3072, s3)))
        if (t == 12) put(1);
    }
    o[3] = MFMA(vb[3][1], pb, o[3]);
    put(2);
    f32x16 ob = o[3];
    const u32x4 qb0 = q[3][0], qb1 = q[3][1];
    asm volatile("s_waitcnt lgkmcnt(0)" ::: "memory");
    lds_add(lds, C_PART); lds_wait<0>(lds, C_PART, 4);
#pragma unroll
    for (int sl = 0; sl < 3; ++sl) {
        const char* src = lds + LDS_PART + (w * 3 + sl) * 4096 + lo;
#pragma unroll
        for (int g = 0; g < 4; ++g) { const float4 x = *(const float4*)(src + g * 1024); ob[4 * g] += x.x; ob[4 * g + 1] += x.y; ob[4 * g + 2] += x.z; ob[4 * g + 3] += x.w; }
    }
    float lb = rowsum(ob);
    if (unsafe(ob, lb)) { ob = exact_tile(rskv, kvB, qb0, qb1, 0, NT - 1, tq, false, lane); lb = rowsum(ob); }
    store_o(ob, lb, rsq, qoB, lane);
    asm volatile("s_waitcnt vmcnt(0)" ::: "memory");
    if (lds_add(lds, C_XOS) == 3) publish(ws, 768 + j * 16 + head, tag);

    if (pr < 28 && ob_ == 1) {
        if (w == 0) {
            wait_flags<4>(oflags + (256 + otb * 16) * FS, lane & 15, true, tag);
            if (lane == 0) *(lds_word)(lds + C_OOK) = 1;
        } else lds_wait<4>(lds, C_OOK, 1);
        out_tile(lds, out, rsq, 1, otb, og, w, lane);
    }
    if (id == 0 && w == 0) {
#pragma unroll
        for (int i = 0; i < 4; ++i) wait_flags<4>(flags + 256 * FS, i * 64 + lane, true, tag);
        if (lane == 0) __hip_atomic_store((gu32*)(ws + WS_NONCE), nonce, RLX_AGENT);
    }
    __builtin_amdgcn_s_waitcnt(0);
    __builtin_amdgcn_endpgm();
}

extern "C" void kernel_launch(void* const* d_in, const int* in_sizes, int n_in, void* d_out, int out_size, void* d_ws, size_t ws_size, hipStream_t stream) {
    (void)hipFuncSetAttribute((const void*)k_main, hipFuncAttributeMaxDynamicSharedMemorySize, LDS_BYTES);
    k_main<<<256, 512, LDS_BYTES, stream>>>(d_in[0], d_in[1], d_in[2], d_in[3], d_in[4], d_in[5], (char*)d_ws, d_out);
}
```

```cpp
#include <hip/hip_runtime.h>
#include <hip/hip_bf16.h>
#include <stdint.h>

typedef float f32x16 __attribute__((ext_vector_type(16)));
typedef float f32x2 __attribute__((ext_vector_type(2)));
typedef __bf16 bf16x8 __attribute__((ext_vector_type(8)));
typedef __bf16 bf16x2 __attribute__((ext_vector_type(2)));
typedef unsigned u32x4 __attribute__((ext_vector_type(4)));
typedef unsigned u32x2 __attribute__((ext_vector_type(2)));
typedef __attribute__((address_space(3))) char* lds_ptr;
typedef __attribute__((address_space(3))) const char* lds_cptr;
typedef __attribute__((address_space(1))) unsigned long long gu64;
typedef __attribute__((address_space(1))) unsigned gu32;

#define DI __device__ __forceinline__
#define SB() __builtin_amdgcn_sched_barrier(0)
#define PIN(x) asm volatile("" : "+v"(x))
#define MFMA(a, b, c) __builtin_amdgcn_mfma_f32_32x32x16_bf16(__builtin_bit_cast(bf16x8, a), __builtin_bit_cast(bf16x8, b), c, 0, 0, 0)
#define RLX_AGENT __ATOMIC_RELAXED, __HIP_MEMORY_SCOPE_AGENT

constexpr int SEQ = 2048, DM = 448, NH = 16, DH = 28, NQKV = 3 * DM;
constexpr int NT = SEQ / 32;
constexpr float QSCALE = 0.18898223650461362f * 1.4426950408889634f;
constexpr float DELTA = 96.f;
constexpr float NEGBIG = -1.0e30f;
constexpr int SC1 = 16;
constexpr unsigned WS_Q = 0, WS_Q_BYTES = 2u * NH * NT * 2048u;
constexpr unsigned WS_KV = WS_Q + WS_Q_BYTES, WS_KV_BYTES = 2u * NH * NT * 4096u;
constexpr unsigned WS_O = WS_Q, WS_O_BYTES = WS_Q_BYTES;
constexpr unsigned WS_FLAG = WS_KV + WS_KV_BYTES;
constexpr int FS = 16;
constexpr unsigned WS_OFLAG = WS_FLAG + 2 * 256 * FS * 8;
constexpr unsigned WS_NONCE = WS_OFLAG + 2 * 256 * FS * 8;
constexpr int RSRC3 = 0x00020000;

#define STAMP(k, i)
#define STAMPC(k, i)

#define PFW 5
#define LATE89 0
#define AHI 0
#define TLBPF 1
#define FULLFLAG 1
#define FLAGPF 1
DI unsigned rfl(unsigned x) { return __builtin_amdgcn_readfirstlane(x); }
DI unsigned cvtpk(float lo, float hi) { f32x2 v = {lo, hi}; bf16x2 b = __builtin_convertvector(v, bf16x2); return __builtin_bit_cast(unsigned, b); }
DI __amdgpu_buffer_rsrc_t mkrs(const void* p, unsigned bytes) { return __builtin_amdgcn_make_buffer_rsrc((void*)p, (short)0, (int)bytes, RSRC3); }
template <int AUX> DI u32x4 bld(__amdgpu_buffer_rsrc_t rs, unsigned voff, unsigned soff) { return __builtin_amdgcn_raw_buffer_load_b128(rs, (int)voff, (int)soff, AUX); }
template <int AUX> DI void bst(u32x4 v, __amdgpu_buffer_rsrc_t rs, unsigned voff, unsigned soff) { __builtin_amdgcn_raw_buffer_store_b128(v, rs, (int)(voff + soff), 0, AUX); }
DI void dma16(__amdgpu_buffer_rsrc_t rs, unsigned voff, unsigned soff, unsigned ldsaddr) {
    asm volatile("s_mov_b32 m0, %0\n\ts_nop 0\n\tbuffer_load_dwordx4 %1, %2, %3 offen lds" ::"s"(ldsaddr), "v"(voff), "s"(rs), "s"(soff) : "m0", "memory");
}
DI u32x2 trread(lds_cptr p) {
    typedef short v4s __attribute__((ext_vector_type(4)));
    return __builtin_bit_cast(u32x2, __builtin_amdgcn_ds_read_tr16_b64_v4i16((__attribute__((address_space(3))) v4s*)p));
}
DI u32x4 ldsread(lds_cptr p) { return *(const __attribute__((address_space(3))) u32x4*)p; }
DI float swapmax(float m) { auto r = __builtin_amdgcn_permlane32_swap(__float_as_uint(m), __float_as_uint(m), false, false); return fmaxf(__uint_as_float(r[0]), __uint_as_float(r[1])); }
DI float max16(const f32x16& s) {
    float a = fmaxf(fmaxf(s[0], s[1]), s[2]), b = fmaxf(fmaxf(s[3], s[4]), s[5]);
    a = fmaxf(fmaxf(a, s[6]), s[7]); b = fmaxf(fmaxf(b, s[8]), s[9]); a = fmaxf(fmaxf(a, s[10]), s[11]); b = fmaxf(fmaxf(b, s[12]), s[13]);
    a = fmaxf(fmaxf(a, s[14]), s[15]); return fmaxf(a, b);
}
DI int crow(int i, int h) { return (i & 3) + 8 * (i >> 2) + 4 * h; }
DI float ex2(float x) { return __builtin_amdgcn_exp2f(x); }

DI unsigned long long mktag(unsigned nonce) { return (unsigned long long)nonce | ((unsigned long long)(~nonce ^ 0x5bd1e995u) << 32); }
DI void publish(char* ws, unsigned line_off, unsigned long long tag) {
    if (FULLFLAG) {
        if ((threadIdx.x & 63) < 8) { const u32x4 v = {(unsigned)tag, (unsigned)(tag >> 32), (unsigned)tag, (unsigned)(tag >> 32)};
            bst<SC1>(v, mkrs(ws + WS_FLAG, 4 * 256 * FS * 8), (threadIdx.x & 63) * 16, line_off); }
    } else if ((threadIdx.x & 63) == 0) __hip_atomic_store((gu64*)(ws + WS_FLAG + line_off), tag, RLX_AGENT);
}
template <int SLEEP> DI void wait_flags(const gu64* flags, unsigned idx, bool need, unsigned long long tag) {
    for (unsigned spins = 0; spins < (1u << 22); ++spins) {
        const unsigned long long x = __hip_atomic_load(flags + idx * FS, RLX_AGENT);
        if (__all(!need || x == tag)) break;
        __builtin_amdgcn_s_sleep(SLEEP);
    }
    asm volatile("" ::: "memory");
}

DI f32x16 exact_tile(__amdgpu_buffer_rsrc_t rskv, unsigned kvbase, u32x4 q0, u32x4 q1, int t0, int t1, int tq, bool causal, int lane) {
    const int r32 = lane & 31, h = lane >> 5;
    if (h) q1[2] = 0;
    float m = NEGBIG;
    for (int t = t0; t <= t1; ++t) {
        const u32x4 k0 = bld<SC1>(rskv, lane * 16, kvbase + t * 4096), k1 = bld<SC1>(rskv, lane * 16, kvbase + t * 4096 + 1024);
        f32x16 s = {}; s = MFMA(k0, q0, s); s = MFMA(k1, q1, s);
        if (causal) {
#pragma unroll
            for (int i = 0; i < 16; ++i) { const int d = (tq - t) * 32 + r32 - crow(i, h); if (d < 0 || d >= 256) s[i] = NEGBIG; }
        }
        m = fmaxf(m, max16(s));
    }
    m = swapmax(m);
    f32x16 o = {};
    for (int t = t0; t <= t1; ++t) {
        const u32x4 k0 = bld<SC1>(rskv, lane * 16, kvbase + t * 4096), k1 = bld<SC1>(rskv, lane * 16, kvbase + t * 4096 + 1024);
        const u32x4 v0 = bld<SC1>(rskv, lane * 16, kvbase + t * 4096 + 2048), v1 = bld<SC1>(rskv, lane * 16, kvbase + t * 4096 + 3072);
        f32x16 s = {}; s = MFMA(k0, q0, s); s = MFMA(k1, q1, s);
        u32x4 pa, pb;
#pragma unroll
        for (int i = 0; i < 16; ++i) {
            s[i] = ex2(s[i] - m);
            if (causal) { const int d = (tq - t) * 32 + r32 - crow(i, h); if (d < 0 || d >= 256) s[i] = 0.f; }
        }
#pragma unroll
        for (int i = 0; i < 4; ++i) { pa[i] = cvtpk(s[2 * i], s[2 * i + 1]); pb[i] = cvtpk(s[8 + 2 * i], s[8 + 2 * i + 1]); }
        o = MFMA(v0, pa, o); o = MFMA(v1, pb, o);
    }
    return o;
}
DI float rowsum(const f32x16& o) { auto r = __builtin_amdgcn_permlane32_swap(__float_as_uint(o[12]), __float_as_uint(o[12]), false, false); return __uint_as_float(r[1]); }
DI bool unsafe(const f32x16& o, float l) {
    float z = 0.f;
#pragma unroll
    for (int i = 0; i < 16; ++i) z = __builtin_fmaf(o[i], 0.f, z);
    const bool ok = (z == 0.f) && (l > 0x1p-106f) && (l < 0x1p100f);
    return __any(!ok);
}
DI void store_o(const f32x16& o, float l, __amdgpu_buffer_rsrc_t rso, unsigned off, int lane) {
    const float rl = __builtin_amdgcn_rcpf(l); u32x4 a, c2;
#pragma unroll
    for (int i = 0; i < 4; ++i) { a[i] = cvtpk(o[2 * i] * rl, o[2 * i + 1] * rl); c2[i] = cvtpk(o[8 + 2 * i] * rl, o[8 + 2 * i + 1] * rl); }
    if (lane >> 5) { c2[2] = 0; c2[3] = 0; }
    bst<SC1>(a, rso, lane * 16, off); bst<SC1>(c2, rso, lane * 16, off + 1024);
}
DI unsigned standin(u32x4 k0, u32x4 k1, u32x4 q0, u32x4 q1, bool causal, int lane) {
    f32x16 s = {}; s = MFMA(k0, q0, s); s = MFMA(k1, q1, s);
    if (causal) {
#pragma unroll
        for (int i = 0; i < 16; ++i) if (crow(i, lane >> 5) > (lane & 31)) s[i] = NEGBIG;
    }
    const float m = swapmax(max16(s));
    return (lane >> 5) ? cvtpk(-(m + DELTA), 0.f) : q1[2];
}

#define LPRIO_P 0
#define LPRIO_A 1
#define BSLEEP 4
#define OSLEEP 4
#define OUTAUX 16
#define PREF 1
#define PREFD 24
#define NPRE 5
#define POLL1 1
#define XPRIO_T 0
#define CODEPF 32000
#define WSEND 0
#define WSOFF(sz) (WSEND ? (((sz) - (40u << 20)) & ~(size_t)((2u << 20) - 1)) : 0)
#define LAZY 1
#define JGMAP 1
#define EARLY 2
#define YHOLD 1
#define YPRIO_P 1
#define XPRIO 0
#define YPRIO 0
constexpr int P_SLOT = 28672, P_XB = 16384, P_WP = 192, P_NS = 5;
constexpr int LDS_CTL = P_NS * P_SLOT, LDS_QX = LDS_CTL + 1024, LDS_PART = 0, LDS_BYTES = LDS_QX + 8192;
typedef volatile __attribute__((address_space(3))) unsigned* lds_word;
constexpr int LDS_OST = 107520;
constexpr int LDS_WO = 49152;
constexpr int C_XOS = LDS_CTL + 128, C_YOS = LDS_CTL + 144, C_OOK = LDS_CTL + 160, C_XEND = LDS_CTL + 176, C_WO = LDS_CTL + 192, C_XIS = LDS_CTL + 208, C_NONCE = LDS_CTL + 224;
constexpr int C_XST = LDS_CTL, C_AOK = LDS_CTL + 16, C_QX = LDS_CTL + 32, C_PART = LDS_CTL + 48, C_BOK = LDS_CTL + 64, C_XRD = LDS_CTL + 80, C_YST = LDS_CTL + 96, C_YBAR = LDS_CTL + 112;

#define EXPCVT(S, b4, P, i) do { const float e0_ = ex2(S[b4]), e1_ = ex2(S[b4 + 1]), e2_ = ex2(S[b4 + 2]), e3_ = ex2(S[b4 + 3]); \
        P[i] = cvtpk(e0_, e1_); P[i + 1] = cvtpk(e2_, e3_); PIN(P); } while (0)

DI unsigned lds_add(char* lds, int off) {
    unsigned old = 0;
    if ((threadIdx.x & 63) == 0) old = __hip_atomic_fetch_add((__attribute__((address_space(3))) unsigned*)(lds + off), 1u, __ATOMIC_RELAXED, __HIP_MEMORY_SCOPE_WORKGROUP);
    return rfl(old);
}
template <int SLEEP> DI void lds_wait(char* lds, int off, unsigned n) {
    lds_word p = (lds_word)(lds + off);
#pragma unroll 1
    for (unsigned spins = 0; spins < (1u << 20) && *p < n; ++spins) __builtin_amdgcn_s_sleep(SLEEP);
    asm volatile("" ::: "memory");
}

struct Proj {
    unsigned ax[4], aw[3];
    f32x16 acc[3];
    u32x4 xf[4], wf[4][3];
    u32x4 id0, id1;
};
DI void p_rdx(Proj& p, lds_cptr L, int buf, int slot, int ks) { p.xf[buf] = ldsread(L + slot * P_SLOT + p.ax[ks]); }
DI void p_rdw(Proj& p, lds_cptr L, int buf, int slot, int ks, int c) {
    lds_cptr wp = L + slot * P_SLOT + p.aw[c] + ks * (16 * P_WP);
    const u32x2 a = trread(wp), b2 = trread(wp + 4 * P_WP);
    p.wf[buf][c] = (u32x4){a[0], a[1], b2[0], b2[1]};
}
template <class Side>
DI void p_kstep(Proj& p, lds_cptr L, int buf, bool first, int nb, bool rd, int slot, int ks2, Side side) {
    const f32x16 zero = {};
    p.acc[0] = MFMA(p.wf[buf][0], p.xf[buf], first ? zero : p.acc[0]); SB(); if (rd) { p_rdx(p, L, nb, slot, ks2); p_rdw(p, L, nb, slot, ks2, 0); } side(0); SB();
    p.acc[1] = MFMA(p.wf[buf][1], p.xf[buf], first ? zero : p.acc[1]); SB(); if (rd) { p_rdw(p, L, nb, slot, ks2, 1); } side(1); SB();
    p.acc[2] = MFMA(p.wf[buf][2], p.xf[buf], first ? zero : p.acc[2]); SB(); if (rd) { p_rdw(p, L, nb, slot, ks2, 2); } side(2); SB();
}
DI void p_setup(Proj& p, int w, int lane, int cb0) {
    const int r32 = lane & 31, h = lane >> 5;
#pragma unroll
    for (int ks = 0; ks < 4; ++ks) p.ax[ks] = (32 * w + r32) * 128 + ((((2 * ks) | h) ^ ((r32 >> 1) & 7)) << 4);
    const int nh = (lane >> 4) & 1, q = (lane & 15) >> 2, pq = lane & 3, pqs = ((pq & 1) << 1) | (pq >> 1);
#pragma unroll
    for (int c = 0; c < 3; ++c) p.aw[c] = P_XB + (8 * h + q) * P_WP + (cb0 & 8) + 56 * c + 32 * nh + 8 * pqs;
#pragma unroll
    for (int e = 0; e < 4; ++e) {
        const int d0 = 8 * h + 2 * e;
        p.id0[e] = (r32 == d0 ? 0x3F80u : 0u) | (r32 == d0 + 1 ? 0x3F800000u : 0u);
        p.id1[e] = (r32 == 16 + d0 ? 0x3F80u : 0u) | (r32 == 17 + d0 ? 0x3F800000u : 0u);
    }
    PIN(p.id0); PIN(p.id1);
#pragma unroll
    for (int i = 0; i < 4; ++i) PIN(p.ax[i]);
#pragma unroll
    for (int i = 0; i < 3; ++i) PIN(p.aw[i]);
}
template <int S0, class Sync, class Piece>
DI void p_run(Proj& p, lds_cptr L, Sync sync, Piece piece) {
    constexpr int sl0 = S0 % P_NS;
    p_rdx(p, L, 0, sl0, 0); p_rdw(p, L, 0, sl0, 0, 0); p_rdw(p, L, 0, sl0, 0, 1); p_rdw(p, L, 0, sl0, 0, 2);
    p_rdx(p, L, 1, sl0, 1); p_rdw(p, L, 1, sl0, 1, 0); p_rdw(p, L, 1, sl0, 1, 1); p_rdw(p, L, 1, sl0, 1, 2); SB();
#pragma unroll
    for (int sb = 0; sb < 7; ++sb) {
        const int s = S0 + sb, sl = s % P_NS, sn = (s + 1) % P_NS;
        p_kstep(p, L, 0, sb == 0, 2, true, sl, 2, [&](int i) { if (sb > 0 && i < 2) piece(s - 1, 5 + i); });
        p_kstep(p, L, 1, false, 3, true, sl, 3, [&](int) {});
        p.acc[0] = MFMA(p.wf[2][0], p.xf[2], p.acc[0]); SB();
        sync(s);
        if (sb < 6) { p_rdx(p, L, 0, sn, 0); p_rdw(p, L, 0, sn, 0, 0); } SB();
        p.acc[1] = MFMA(p.wf[2][1], p.xf[2], p.acc[1]); SB(); if (sb < 6) p_rdw(p, L, 0, sn, 0, 1); piece(s, 0); SB();
        p.acc[2] = MFMA(p.wf[2][2], p.xf[2], p.acc[2]); SB(); if (sb < 6) p_rdw(p, L, 0, sn, 0, 2); piece(s, 1); SB();
        p_kstep(p, L, 3, false, 1, sb < 6, sn, 1, [&](int i) { piece(s, 2 + i); });
    }
}
DI void p_store(Proj& p, char* ws, int b, int jg, int tile, int lane) {
    const int h = lane >> 5;
    const f32x16 zero = {};
    const __amdgpu_buffer_rsrc_t rsall = mkrs(ws, WS_Q_BYTES + WS_KV_BYTES);
    u32x4 f[3][2];
#pragma unroll
    for (int c = 0; c < 3; ++c) {
        const int part = (3 * jg + c) >> 4;
        const f32x16 v = p.acc[c] * (part == 0 ? QSCALE : 1.0f);
#pragma unroll
        for (int i = 0; i < 4; ++i) { f[c][0][i] = cvtpk(v[2 * i], v[2 * i + 1]); f[c][1][i] = cvtpk(v[8 + 2 * i], v[8 + 2 * i + 1]); }
        if (h) { f[c][1][2] = part == 0 ? 0u : 0x00003F80u; f[c][1][3] = 0; }
        if (part == 2) { p.acc[c] = MFMA(f[c][0], p.id0, zero); p.acc[c] = MFMA(f[c][1], p.id1, p.acc[c]); }
        else {
            const unsigned th = (b * NH + ((3 * jg + c) & 15)) * NT + tile, o = part == 0 ? WS_Q + th * 2048u : WS_KV + th * 4096u;
            bst<SC1>(f[c][0], rsall, lane * 16, o); bst<SC1>(f[c][1], rsall, lane * 16 + 1024, o);
        }
    }
#pragma unroll
    for (int c = 0; c < 3; ++c) {
        if (((3 * jg + c) >> 4) == 2) {
#pragma unroll
            for (int i = 0; i < 4; ++i) { f[c][0][i] = cvtpk(p.acc[c][2 * i], p.acc[c][2 * i + 1]); f[c][1][i] = cvtpk(p.acc[c][8 + 2 * i], p.acc[c][8 + 2 * i + 1]); }
            const unsigned o = WS_KV + ((b * NH + ((3 * jg + c) & 15)) * NT + tile) * 4096u + 2048u;
            bst<SC1>(f[c][0], rsall, lane * 16, o); bst<SC1>(f[c][1], rsall, lane * 16 + 1024, o);
        }
    }
}

DI void out_tile(char* lds, char* ws, void* out, __amdgpu_buffer_rsrc_t rso, int ob_, int otb, int og, int w, int lane) {
    const int r32 = lane & 31, h = lane >> 5;
    const unsigned lo = lane * 16;
    lds_cptr L = (lds_cptr)lds;
    const f32x16 zero = {};
    const int tile = otb * 4 + w;
    const unsigned obase = (ob_ * NH * NT + tile) * 2048u;
    u32x4 of[32];
#pragma unroll
    for (int i = 0; i < 32; ++i) of[i] = bld<SC1>(rso, lo + (i & 1) * 1024, obase + (i >> 1) * (NT * 2048));
    unsigned aw;
    {
        const int nh = (lane >> 4) & 1, q = (lane & 15) >> 2, pq = lane & 3, pqs = ((pq & 1) << 1) | (pq >> 1);
        const int cb = 32 * nh + 8 * pqs;
        aw = LDS_WO + (4 * h + q) * 128 + (((cb >> 4) ^ (4 * (q >> 1))) << 4) + (cb & 8);
    }
    lds_wait<0>(lds, C_WO, 4);
    f32x16 acc[2];
    u32x4 wf[2][2];
    auto rd = [&](int buf, int ks, int fb) {
        const int hd = ks >> 1, s2 = ks & 1;
        lds_cptr wp = L + ((aw ^ (fb * 64)) + (28 * hd + 16 * s2) * 128);
        const u32x2 x = trread(wp), y = trread(wp + 8 * 128);
        wf[buf][fb] = (u32x4){x[0], x[1], y[0], y[1]};
    };
    rd(0, 0, 0); rd(0, 0, 1); SB();
#pragma unroll
    for (int ks = 0; ks < 32; ++ks) {
        acc[0] = MFMA(wf[ks & 1][0], of[ks], ks == 0 ? zero : acc[0]); SB(); if (ks + 1 < 32) rd((ks + 1) & 1, ks + 1, 0); SB();
        acc[1] = MFMA(wf[ks & 1][1], of[ks], ks == 0 ? zero : acc[1]); SB(); if (ks + 1 < 32) rd((ks + 1) & 1, ks + 1, 1); SB();
    }
    const __amdgpu_buffer_rsrc_t rsout = mkrs(out, SEQ * 2 * DM * 2);
    char* stg = lds + LDS_OST + w * 4096;
#pragma unroll
    for (int fb = 0; fb < 2; ++fb) {
        u32x4 x, y;
#pragma unroll
        for (int i = 0; i < 4; ++i) { x[i] = cvtpk(acc[fb][2 * i], acc[fb][2 * i + 1]); y[i] = cvtpk(acc[fb][8 + 2 * i], acc[fb][8 + 2 * i + 1]); }
        *(u32x4*)(stg + r32 * 128 + (((fb * 4 + h) ^ (r32 & 7)) << 4)) = x;
        *(u32x4*)(stg + r32 * 128 + (((fb * 4 + 2 + h) ^ (r32 & 7)) << 4)) = y;
    }
    const int a = lane >> 3, pc = lane & 7;
#pragma unroll
    for (int k = 0; k < 4; ++k) {
        const u32x4 v = *(const u32x4*)(stg + (8 * k + a) * 128 + ((pc ^ a) << 4));
        bst<OUTAUX>(v, rsout, (tile * 32 + 8 * k + a) * (2 * DM * 2) + pc * 16, ob_ * (DM * 2) + og * 128);
    }
}

__global__ __launch_bounds__(512, 2) void k_main(const void* __restrict__ xA, const void* __restrict__ wA, const void* __restrict__ woA, const void* __restrict__ xB,
                                                 const void* __restrict__ wB, const void* __restrict__ woB, char* __restrict__ ws, void* __restrict__ out) {
    unsigned long long kpc; asm volatile("s_getpc_b64 %0" : "=s"(kpc)); kpc &= ~255ull;
    extern __shared__ __attribute__((aligned(16))) char lds[];
    const int tid = threadIdx.x, lane = tid & 63, wv = rfl(tid >> 6), w = wv & 3;
    const int id = blockIdx.x;
    const int pc_ = id & 7, pr = id >> 3;
    const int tb = (pc_ & 3) * 4 + (pr & 3);
    const int jg = JGMAP ? (int)(((pc_ < 4 ? 0xBA54C963u : 0xFED87210u) >> (4 * (pr >> 2))) & 15) : (pc_ >> 2) * 8 + (pr >> 2);
    const int head = 2 * (id & 7) + (id >> 7), j = (id >> 3) & 15;
    gu64* flags = (gu64*)(ws + WS_FLAG);
    const unsigned lds0 = (unsigned)(uintptr_t)lds;
    const int r32 = lane & 31, h = lane >> 5;
    const unsigned lo = lane * 16;
    const __amdgpu_buffer_rsrc_t rsq = mkrs(ws + WS_Q, WS_Q_BYTES), rskv = mkrs(ws + WS_KV, WS_KV_BYTES), rso = mkrs(ws + WS_O, WS_O_BYTES);
    const int cb0 = 168 * jg;
    const int gk = (16 + head) / 3, gv = (32 + head) / 3, gq = head / 3;
    const int tq = 4 * j + w;
    const int ob_ = pc_ >> 2, otb = (pc_ & 3) * 4 + (pr & 3), og = pr >> 2;
    const f32x16 zero = {};
    lds_cptr L = (lds_cptr)lds;
    STAMP(wv >> 2, 0);
    const __amdgpu_buffer_rsrc_t rsxB = mkrs(xB, SEQ * DM * 2), rswB = mkrs(wB, DM * NQKV * 2), rsxA = mkrs(xA, SEQ * DM * 2), rswA = mkrs(wA, DM * NQKV * 2);
    unsigned vx[4] = {}, vw[3] = {};
    auto stage = [&](int s) {
        const unsigned slot = lds0 + (s % P_NS) * P_SLOT;
        const int sb = s >= 7 ? s - 7 : s;
        const __amdgpu_buffer_rsrc_t rx = s >= 7 ? rsxA : rsxB, rw = s >= 7 ? rswA : rswB;
#pragma unroll
        for (int i = 0; i < 4; ++i) dma16(rx, vx[i], rfl(sb * 128), rfl(slot + (4 * w + i) * 1024));
#pragma unroll
        for (int i = 0; i < 3; ++i) dma16(rw, vw[i], rfl(sb * 64 * NQKV * 2), rfl(slot + P_XB + (3 * w + i) * 1024));
    };
    if (wv >= 4) {
        {
            const int a = lane >> 3, pc = lane & 7;
#pragma unroll
            for (int i = 0; i < 4; ++i) { const int row = tb * 128 + 32 * w + 8 * i + a; vx[i] = row * (DM * 2) + ((pc ^ (((a >> 1) | (4 * (i & 1))))) << 4); }
#pragma unroll
            for (int i = 0; i < 3; ++i) {
                const int u = 64 * (3 * w + i) + lane, row = u / 12, pp = u - 12 * row;
                vw[i] = row * (NQKV * 2) + (cb0 & ~15) + (pp > 10 ? 10 : pp) * 16;
            }
        }
        stage(0); stage(1);
    }
    int role = wv; asm volatile("" : "+s"(role));
    if (role >= 4) {
        Proj p; p_setup(p, w, lane, cb0);
        auto piece1 = [&](int s, int n) {
            const unsigned slot = lds0 + (s % P_NS) * P_SLOT;
            const int sb = s - 7;
            if (n < 4) dma16(rsxA, vx[n], rfl(sb * 128), rfl(slot + (4 * w + n) * 1024));
            else dma16(rswA, vw[n - 4], rfl(sb * 64 * NQKV * 2), rfl(slot + P_XB + (3 * w + n - 4) * 1024));
        };
#define L_WAIT(n) asm volatile("s_waitcnt vmcnt(" #n ")\n\ts_barrier" ::: "memory")
        L_WAIT(7);
        stage(2); L_WAIT(7);
        stage(3); L_WAIT(7);
        stage(4); L_WAIT(7);
        stage(5); L_WAIT(7);
        stage(6); L_WAIT(7);
        stage(7); L_WAIT(7);
        if (LATE89 == 0) { stage(8); stage(9); }
        if (LATE89 == 2) stage(8);
        STAMP(1, 1);
        __builtin_amdgcn_s_setprio(YPRIO_P);
        unsigned ybar = 0;
        auto ymeet = [&]() { asm volatile("s_waitcnt lgkmcnt(0)" ::: "memory"); lds_add(lds, C_YBAR); ybar += 4; lds_wait<0>(lds, C_YBAR, ybar); };
        lds_wait<1>(lds, EARLY == 2 ? C_XIS : C_XRD, 4);
        if (LATE89 == 1) { stage(8); stage(9); }
        if (LATE89 == 2) stage(9);
        stage(10); stage(11);
        asm volatile("s_waitcnt vmcnt(14)" ::: "memory"); ymeet();
        p_run<7>(p, L, [&](int s) {
            if (s == 8 || s == 11) { asm volatile("s_waitcnt vmcnt(0)" ::: "memory"); ymeet(); }
        }, [&](int s, int n) {
            if (s == 8) { piece1(12 + (2 * n) / 7, (2 * n) % 7); piece1(12 + (2 * n + 1) / 7, (2 * n + 1) % 7); }
        });
        STAMP(1, 2);
        p_store(p, ws, 0, jg, tb * 4 + w, lane);
        const unsigned long long tag = mktag(*(lds_word)(lds + C_NONCE) + 1u);
        asm volatile("s_waitcnt vmcnt(0)" ::: "memory");
        if (lds_add(lds, C_YST) == 3) publish(ws, (jg * 16 + tb) * (FS * 8), tag);
        STAMP(1, 3);
        if (pr < 28) {
            lds_wait<1>(lds, C_YST, 4);
            const __amdgpu_buffer_rsrc_t rsw = mkrs(ob_ ? woB : woA, DM * DM * 2);
            const int a = lane >> 3, pc = lane & 7;
            const unsigned v = a * (DM * 2) + og * 128 + ((pc ^ (4 * ((a >> 1) & 1))) << 4);
#pragma unroll 1
            for (int i = w; i < 57; i += 4) dma16(rsw, v, rfl((i < 56 ? i : 55) * 8 * DM * 2), rfl(lds0 + LDS_WO + i * 1024));
            asm volatile("s_waitcnt vmcnt(0)" ::: "memory");
            lds_add(lds, C_WO);
        }
        if (w == 0) {
            const int a3 = lane % 3, blk = j - a3;
            const unsigned idx = lane < 3 ? gk * 16 + blk : lane < 6 ? gv * 16 + blk : gq * 16 + j;
            wait_flags<8>(flags, (blk < 0 && lane < 6) ? 0 : idx, lane <= 6 && !(blk < 0 && lane < 6), tag);
            if (lane == 0) *(lds_word)(lds + C_AOK) = 1;
        } else {
            lds_wait<8>(lds, C_AOK, 1);
        }
        __builtin_amdgcn_s_setprio(YPRIO);
        STAMP(1, 4);
        const unsigned kvA = head * NT * 4096u, qoA = (head * NT + tq) * 2048u;
        const u32x4 qa0 = bld<SC1>(rsq, lo, qoA); u32x4 qa1 = bld<SC1>(rsq, lo + 1024, qoA);
        f32x16 oa = {};
        const int t0 = max(tq - 8, 0);
        {
            u32x4 kb[9][2], vb[9][2];
#pragma unroll
            for (int u = 0; u < 9; ++u) {
                const unsigned so = (tq - 8 + u >= 0) ? kvA + (tq - 8 + u) * 4096u : WS_KV_BYTES;
                kb[u][0] = bld<SC1>(rskv, lo, so); kb[u][1] = bld<SC1>(rskv, lo + 1024, so); vb[u][0] = bld<SC1>(rskv, lo + 2048, so); vb[u][1] = bld<SC1>(rskv, lo + 3072, so);
            }
            f32x16 cd, cm;
#pragma unroll
            for (int i = 0; i < 16; ++i) { cd[i] = (crow(i, h) > r32) ? NEGBIG : 0.f; cm[i] = __uint_as_float(__float_as_uint(cd[i]) ^ __float_as_uint(NEGBIG)); }
            {
                f32x16 s = MFMA(kb[8][0], qa0, cd); s = MFMA(kb[8][1], qa1, s);
                const float m = swapmax(max16(s));
                if (h) qa1[2] = cvtpk(-(m + DELTA), 0.f);
            }
            f32x16 sA, sB; u32x4 pa, pb;
            sA = MFMA(kb[0][0], qa0, cm); sA = MFMA(kb[0][1], qa1, sA);
#define AUNIT(SC, SN, U)                                                                                                  \
            { if ((U) < 8) { SN = MFMA(kb[((U) + 1) % 9][0], qa0, ((U) == 7 ? cd : zero)); } SB();                         \
              EXPCVT(SC, 0, pa, 0); SB();                                                                                 \
              if ((U) < 8) { SN = MFMA(kb[((U) + 1) % 9][1], qa1, SN); } SB();                                             \
              EXPCVT(SC, 4, pa, 2); SB();                                                                                 \
              if ((U) > 0) { oa = MFMA(vb[((U) + 8) % 9][1], pb, oa); } SB();                                              \
              EXPCVT(SC, 8, pb, 0); SB();                                                                                 \
              oa = MFMA(vb[U][0], pa, oa); SB();                                                                          \
              EXPCVT(SC, 12, pb, 2); SB(); }
#define APR(U) if (AHI > 0 && (U) == AHI) __builtin_amdgcn_s_setprio(0);
            if (AHI > 0) __builtin_amdgcn_s_setprio(1);
            AUNIT(sA, sB, 0) APR(1) AUNIT(sB, sA, 1) APR(2) AUNIT(sA, sB, 2) APR(3) AUNIT(sB, sA, 3) APR(4) AUNIT(sA, sB, 4) APR(5) AUNIT(sB, sA, 5) APR(6) AUNIT(sA, sB, 6) APR(7) AUNIT(sB, sA, 7) APR(8) AUNIT(sA, sB, 8)
            oa = MFMA(vb[8][1], pb, oa);
        }
        float la = rowsum(oa);
        if (unsafe(oa, la)) { oa = exact_tile(rskv, kvA, qa0, qa1, t0, tq, tq, true, lane); la = rowsum(oa); }
        store_o(oa, la, rso, (head * NT + tq) * 2048u, lane);
        asm volatile("s_waitcnt vmcnt(0)" ::: "memory");
        gu64* oflags = (gu64*)(ws + WS_OFLAG);
        if (lds_add(lds, C_YOS) == 3) publish(ws, (512 + j * 16 + head) * (FS * 8), tag);
        STAMP(1, 5);
        if (pr >= 28 || ob_ != 0) __builtin_amdgcn_endpgm();
        __builtin_amdgcn_s_setprio(0);
        if (w == 0) {
            wait_flags<OSLEEP>(oflags + otb * 16 * FS, lane & 15, true, tag);
            if (lane == 0) *(lds_word)(lds + C_OOK) = 1;
        } else lds_wait<OSLEEP>(lds, C_OOK, 1);
        STAMP(1, 6);
        out_tile(lds, ws, out, rso, 0, otb, og, w, lane);
        STAMP(1, 8);
        __builtin_amdgcn_endpgm();
    }
    if (tid == 0) { for (int i = 0; i < 14; ++i) *(lds_word)(lds + LDS_CTL + 16 * i) = 0; }
    unsigned pf0 = 0, pf1 = 0;
    if (PREF) {
        const int gw = id * 4 + w;
        if (PREFD && gw >= 371) __builtin_amdgcn_s_sleep(PREFD);
        if (gw < 840) {
            const char* base; int c = gw;
            if (c < 224) base = (const char*)xB; else if ((c -= 224) < 147) base = (const char*)wB; else if ((c -= 147) < 224) base = (const char*)xA;
            else if ((c -= 224) < 147) base = (const char*)wA; else if ((c -= 147) < 49) base = (const char*)woA; else { c -= 49; base = (const char*)woB; }
            const char* a = base + c * 8192 + lane * 64;
            asm volatile("global_load_dword %0, %2, off\n\tglobal_load_dword %1, %3, off" : "=&v"(pf0), "=&v"(pf1) : "v"(a), "v"(a + 4096) : "memory");
        }
    }
    unsigned pf3 = 0;
    if (TLBPF && w == 2 && lane < 42) {
        const char* a = lane < 19 ? (const char*)wB + lane * 65536 : lane < 38 ? (const char*)wA + (lane - 19) * 65536
                      : (lane < 40 ? (const char*)xB : (const char*)xA) + tb * (128 * DM * 2) + (lane & 1) * 65536;
        asm volatile("global_load_dword %0, %1, off" : "=&v"(pf3) : "v"(a) : "memory");
    }
    unsigned pf2 = 0;
    if (FLAGPF && w == 1 && lane < 4) { const char* a = ws + WS_FLAG + (id * 4 + lane) * (FS * 8); asm volatile("global_load_dword %0, %1, off" : "=&v"(pf2) : "v"(a) : "memory"); }
    unsigned nonce;
    { const char* a = ws + WS_NONCE; asm volatile("global_load_dword %0, %1, off sc1" : "=&v"(nonce) : "v"(a) : "memory"); }
    unsigned pc0 = 0;
    if (CODEPF) {
        const unsigned off = (pr * 4 + w) * 256 + lane * 64;
        if (lane < 4 && off < CODEPF) { const char* a = (const char*)kpc + off; asm volatile("global_load_dword %0, %1, off" : "=&v"(pc0) : "v"(a) : "memory"); }
    }
    {
        Proj p; p_setup(p, w, lane, cb0);
#define P_BAR() asm volatile("s_waitcnt lgkmcnt(0)\n\ts_barrier" ::: "memory")
        P_BAR();
        STAMP(0, 1);
        p_run<0>(p, L, [&](int s) {
            if (s == PFW) { asm volatile("s_waitcnt vmcnt(0)" : "+v"(pf0), "+v"(pf1), "+v"(pc0), "+v"(pf2), "+v"(pf3), "+v"(nonce) :: "memory");
                if (tid == 0) *(lds_word)(lds + C_NONCE) = nonce; }
            if (s < 6) P_BAR();
            else { asm volatile("s_waitcnt lgkmcnt(0)" ::: "memory"); lds_add(lds, C_XRD); }
        }, [&](int, int) {});
        STAMP(0, 2);
        p_store(p, ws, 1, jg, tb * 4 + w, lane);
    }
    if (YHOLD == 2 || EARLY == 2) lds_add(lds, C_XIS);
    STAMP(0, 3);
    nonce += 1u;
    const unsigned long long tag = mktag(nonce);
    asm volatile("s_waitcnt vmcnt(0)" ::: "memory");
    if (lds_add(lds, C_XST) == 3) publish(ws, (256 + jg * 16 + tb) * (FS * 8), tag);
    STAMP(0, 4);
    const unsigned kvB = (NH + head) * NT * 4096u, kvw = kvB + 16 * w * 4096u;
    u32x4 q[4][2], kb[4][2], vb[4][2];
    const gu64* fB = flags + 256 * FS + (lane < 8 ? (lane < 4 ? gk : gv) * 16 + 4 * w + (lane & 3) : lane == 8 ? gq * 16 + j : gk * 16 + j) * FS;
    unsigned long long seen = 0;
#pragma unroll 1
    for (unsigned spins = 0; spins < (1u << 17); ++spins) {
        seen = __ballot(__hip_atomic_load(fB, RLX_AGENT) == tag);
        if ((seen & (LAZY == 2 ? 0x311 : 0x300)) == (LAZY == 2 ? 0x311 : 0x300)) break;
        __builtin_amdgcn_s_sleep(BSLEEP);
    }
    asm volatile("" ::: "memory");
    const unsigned qo = ((NH + head) * NT + tq) * 2048u;
    u32x4 q0 = bld<SC1>(rsq, lo, qo), q1 = bld<SC1>(rsq, lo + 1024, qo);
    const u32x4 kd0 = bld<SC1>(rskv, lo, kvB + tq * 4096u), kd1 = bld<SC1>(rskv, lo + 1024, kvB + tq * 4096u);
    SB();
#pragma unroll 1
    for (unsigned spins = 0; spins < (1u << 17) && (seen & 0x11) != 0x11; ++spins) {
        seen = __ballot(__hip_atomic_load(fB, RLX_AGENT) == tag);
        if ((seen & 0x11) != 0x11) __builtin_amdgcn_s_sleep(BSLEEP);
    }
    asm volatile("" ::: "memory");
    STAMP(0, 5);
    kb[0][0] = bld<SC1>(rskv, lo, kvw); kb[0][1] = bld<SC1>(rskv, lo + 1024, kvw); vb[0][0] = bld<SC1>(rskv, lo + 2048, kvw); vb[0][1] = bld<SC1>(rskv, lo + 3072, kvw);
    unsigned long long x2 = tag;
    if ((seen & 0xFF) != 0xFF) x2 = __hip_atomic_load(fB, RLX_AGENT);
    SB();
    q1[2] = standin(kd0, kd1, q0, q1, false, lane);
    *(u32x4*)(lds + LDS_QX + w * 2048 + lo) = q0; *(u32x4*)(lds + LDS_QX + w * 2048 + 1024 + lo) = q1;
    asm volatile("s_waitcnt lgkmcnt(0)" ::: "memory");
    lds_add(lds, C_QX);
#pragma unroll
    for (int t = 1; t < 3; ++t) { kb[t][0] = bld<SC1>(rskv, lo, kvw + t * 4096); kb[t][1] = bld<SC1>(rskv, lo + 1024, kvw + t * 4096); vb[t][0] = bld<SC1>(rskv, lo + 2048, kvw + t * 4096); vb[t][1] = bld<SC1>(rskv, lo + 3072, kvw + t * 4096); }
    SB();
    lds_wait<0>(lds, C_QX, 4);
#pragma unroll
    for (int i = 0; i < 4; ++i) { const int qb = (w + 1 + i) & 3;
        q[i][0] = *(const u32x4*)(lds + LDS_QX + qb * 2048 + lo); q[i][1] = *(const u32x4*)(lds + LDS_QX + qb * 2048 + 1024 + lo); }
    if ((__ballot(x2 == tag) & 0xFF) != 0xFF) wait_flags<BSLEEP>(fB, 0, lane < 8, tag);
    __builtin_amdgcn_s_setprio(XPRIO);
    vb[3][1] = (u32x4){0, 0, 0, 0};
    f32x16 o[4] = {}; f32x16 sA, sB;
    u32x4 pa, pb = {0, 0, 0, 0};
    sA = MFMA(kb[0][0], q[0][0], zero); sA = MFMA(kb[0][1], q[0][1], sA);
    STAMP(0, 6); STAMPC(0, 28);
#define UNIT(SC, SN, T, B, LD)                                                                                           \
    { constexpr int Tn = ((B) == 3) ? (((T) + 1) & 3) : (T), Bn = ((B) + 1) & 3, Tp = ((B) == 0) ? (((T) + 3) & 3) : (T), Bp = ((B) + 3) & 3; \
      SN = MFMA(kb[Tn][0], q[Bn][0], zero); SB(); EXPCVT(SC, 0, pa, 0); SB();                                         \
      SN = MFMA(kb[Tn][1], q[Bn][1], SN); SB(); EXPCVT(SC, 4, pa, 2); SB();                                           \
      o[Bp] = MFMA(vb[Tp][1], pb, o[Bp]); SB(); LD; SB(); EXPCVT(SC, 8, pb, 0); SB();                                 \
      o[B] = MFMA(vb[T][0], pa, o[B]); SB(); EXPCVT(SC, 12, pb, 2); SB(); }
#define TILE(T, so)                                                                                                      \
    UNIT(sA, sB, T, 0, (kb[((T) + 3) & 3][0] = bld<SC1>(rskv, lo, so)))                                               \
    UNIT(sB, sA, T, 1, (kb[((T) + 3) & 3][1] = bld<SC1>(rskv, lo + 1024, so)))                                        \
    UNIT(sA, sB, T, 2, (vb[((T) + 3) & 3][0] = bld<SC1>(rskv, lo + 2048, so)))                                        \
    UNIT(sB, sA, T, 3, (vb[((T) + 3) & 3][1] = bld<SC1>(rskv, lo + 3072, so)))
    auto put = [&](int i) {
        char* dst = lds + LDS_PART + (((w + 1 + i) & 3) * 3 + i) * 4096 + lo;
#pragma unroll
        for (int g = 0; g < 4; ++g) *(float4*)(dst + g * 1024) = make_float4(o[i][4 * g], o[i][4 * g + 1], o[i][4 * g + 2], o[i][4 * g + 3]);
    };
    unsigned sn = kvw + 3 * 4096u;
#pragma unroll 1
    for (int t = 0; t < 16; t += 4) {
        const unsigned sl = kvw + 15 * 4096u;
        const unsigned s0 = sn, s1 = min(sn + 4096u, sl), s2 = min(sn + 8192u, sl), s3 = min(sn + 12288u, sl); sn += 16384u;
        if (t == 12) lds_wait<2>(lds, C_YST, 4);
        TILE(0, s0) TILE(1, s1) TILE(2, s2)
        UNIT(sA, sB, 3, 0, (kb[2][0] = bld<SC1>(rskv, lo, s3)))
        UNIT(sB, sA, 3, 1, (kb[2][1] = bld<SC1>(rskv, lo + 1024, s3)))
        UNIT(sA, sB, 3, 2, (vb[2][0] = bld<SC1>(rskv, lo + 2048, s3)))
        if (t == 12) put(0);
        SB();
        UNIT(sB, sA, 3, 3, (vb[2][1] = bld<SC1>(rskv, lo + 3072, s3)))
        if (t == 12) put(1);
        STAMP(0, 7 + t / 4);
    }
    STAMPC(0, 29);
    o[3] = MFMA(vb[3][1], pb, o[3]);
    __builtin_amdgcn_s_setprio(XPRIO_T);
    put(2);
    f32x16 ob = o[3];
    const u32x4 qb0 = q[3][0], qb1 = q[3][1];
    asm volatile("s_waitcnt lgkmcnt(0)" ::: "memory");
    lds_add(lds, C_PART); lds_wait<0>(lds, C_PART, 4);
    STAMP(0, 11);
#pragma unroll
    for (int sl = 0; sl < 3; ++sl) {
        const char* src = lds + LDS_PART + (w * 3 + sl) * 4096 + lo;
#pragma unroll
        for (int g = 0; g < 4; ++g) { const float4 x = *(const float4*)(src + g * 1024); ob[4 * g] += x.x; ob[4 * g + 1] += x.y; ob[4 * g + 2] += x.z; ob[4 * g + 3] += x.w; }
    }
    float lb = rowsum(ob);
    if (unsafe(ob, lb)) { ob = exact_tile(rskv, kvB, qb0, qb1, 0, NT - 1, tq, false, lane); lb = rowsum(ob); }
    store_o(ob, lb, rso, ((NH + head) * NT + tq) * 2048u, lane);
    asm volatile("s_waitcnt vmcnt(0)" ::: "memory");
    if (lds_add(lds, C_XOS) == 3) publish(ws, (768 + j * 16 + head) * (FS * 8), tag);
    STAMP(0, 12);
    if (pr < 28 && ob_ == 1) {
        if (w == 0) {
            wait_flags<OSLEEP>((gu64*)(ws + WS_OFLAG) + (256 + otb * 16) * FS, lane & 15, true, tag);
            if (lane == 0) *(lds_word)(lds + C_OOK) = 1;
        } else lds_wait<OSLEEP>(lds, C_OOK, 1);
        STAMP(0, 13);
        out_tile(lds, ws, out, rso, 1, otb, og, w, lane);
        STAMP(0, 14);
    }
    if (id == 0 && w == 0) {
#pragma unroll
        for (int i = 0; i < 4; ++i) wait_flags<4>(flags + 256 * FS, i * 64 + lane, true, tag);
        if (lane == 0) __hip_atomic_store((gu32*)(ws + WS_NONCE), nonce, RLX_AGENT);
    }
    __builtin_amdgcn_s_waitcnt(0);
    __builtin_amdgcn_endpgm();
}

extern "C" void kernel_launch(void* const* d_in, const int* in_sizes, int n_in, void* d_out, int out_size, void* d_ws, size_t ws_size, hipStream_t stream) {
    (void)hipFuncSetAttribute((const void*)k_main, hipFuncAttributeMaxDynamicSharedMemorySize, LDS_BYTES);
    k_main<<<256, 512, LDS_BYTES, stream>>>(d_in[0], d_in[1], d_in[2], d_in[3], d_in[4], d_in[5], (char*)d_ws + WSOFF(ws_size), d_out);
}
```

```cpp
#include <hip/hip_runtime.h>
#include <hip/hip_bf16.h>
#include <stdint.h>

typedef float f32x16 __attribute__((ext_vector_type(16)));
typedef float f32x2 __attribute__((ext_vector_type(2)));
typedef __bf16 bf16x8 __attribute__((ext_vector_type(8)));
typedef __bf16 bf16x2 __attribute__((ext_vector_type(2)));
typedef unsigned u32x4 __attribute__((ext_vector_type(4)));
typedef unsigned u32x2 __attribute__((ext_vector_type(2)));
typedef __attribute__((address_space(3))) const char* lds_cptr;
typedef volatile __attribute__((address_space(3))) unsigned* lds_word;
typedef __attribute__((address_space(1))) unsigned long long gu64;
typedef __attribute__((address_space(1))) unsigned gu32;

#define DI __device__ __forceinline__
#define SB() __builtin_amdgcn_sched_barrier(0)
#define PIN(x) asm volatile("" : "+v"(x))
#define MFMA(a, b, c) __builtin_amdgcn_mfma_f32_32x32x16_bf16(__builtin_bit_cast(bf16x8, a), __builtin_bit_cast(bf16x8, b), c, 0, 0, 0)
#define RLX_AGENT __ATOMIC_RELAXED, __HIP_MEMORY_SCOPE_AGENT

constexpr int SEQ = 2048, DM = 448, NH = 16, NQKV = 3 * DM;
constexpr int NT = SEQ / 32;
constexpr float QSCALE = 0.18898223650461362f * 1.4426950408889634f;
constexpr float DELTA = 96.f;
constexpr float NEGBIG = -1.0e30f;
constexpr int SC1 = 16;
constexpr int RSRC3 = 0x00020000;
constexpr unsigned CODE_TOUCH = 32000;

constexpr unsigned WS_Q = 0, WS_Q_BYTES = 2u * NH * NT * 2048u;
constexpr unsigned WS_KV = WS_Q + WS_Q_BYTES, WS_KV_BYTES = 2u * NH * NT * 4096u;
constexpr int FS = 16;
constexpr unsigned WS_FLAG = WS_KV + WS_KV_BYTES;
constexpr unsigned WS_OFLAG = WS_FLAG + 2 * 256 * FS * 8;
constexpr unsigned WS_NONCE = WS_OFLAG + 2 * 256 * FS * 8;

constexpr int P_SLOT = 28672, P_XB = 16384, P_WP = 192, P_NS = 5;
constexpr int LDS_PART = 0;
constexpr int LDS_WO = 49152;
constexpr int LDS_OST = 107520;
constexpr int LDS_CTL = P_NS * P_SLOT, LDS_QX = LDS_CTL + 1024, LDS_BYTES = LDS_QX + 8192;
constexpr int C_XST = LDS_CTL,
              C_AOK = LDS_CTL + 16,
              C_QX = LDS_CTL + 32,
              C_PART = LDS_CTL + 48,
              C_XIS = LDS_CTL + 64,
              C_YST = LDS_CTL + 80,
              C_YBAR = LDS_CTL + 96,
              C_XOS = LDS_CTL + 112,
              C_YOS = LDS_CTL + 128,
              C_OOK = LDS_CTL + 144,
              C_WO = LDS_CTL + 160,
              C_NONCE = LDS_CTL + 176;
constexpr int N_CTL = 11;
static_assert(LDS_PART + 12 * 4096 <= LDS_WO && LDS_WO + 57 * 1024 <= LDS_OST && LDS_OST + 4 * 4096 <= LDS_CTL && LDS_BYTES <= 160 * 1024, "LDS map");

DI unsigned rfl(unsigned x) { return __builtin_amdgcn_readfirstlane(x); }
DI unsigned cvtpk(float lo, float hi) { f32x2 v = {lo, hi}; bf16x2 b = __builtin_convertvector(v, bf16x2); return __builtin_bit_cast(unsigned, b); }
DI __amdgpu_buffer_rsrc_t mkrs(const void* p, unsigned bytes) { return __builtin_amdgcn_make_buffer_rsrc((void*)p, (short)0, (int)bytes, RSRC3); }
DI u32x4 bld(__amdgpu_buffer_rsrc_t rs, unsigned voff, unsigned soff) { return __builtin_amdgcn_raw_buffer_load_b128(rs, (int)voff, (int)soff, SC1); }
DI void bst(u32x4 v, __amdgpu_buffer_rsrc_t rs, unsigned voff, unsigned soff) { __builtin_amdgcn_raw_buffer_store_b128(v, rs, (int)(voff + soff), 0, SC1); }
DI void dma16(__amdgpu_buffer_rsrc_t rs, unsigned voff, unsigned soff, unsigned ldsaddr) {
    asm volatile("s_mov_b32 m0, %0\n\ts_nop 0\n\tbuffer_load_dwordx4 %1, %2, %3 offen lds" ::"s"(ldsaddr), "v"(voff), "s"(rs), "s"(soff) : "m0", "memory");
}
#define TOUCH(dst, addr) asm volatile("global_load_dword %0, %1, off" : "=&v"(dst) : "v"(addr) : "memory")
DI u32x2 trread(lds_cptr p) {
    typedef short v4s __attribute__((ext_vector_type(4)));
    return __builtin_bit_cast(u32x2, __builtin_amdgcn_ds_read_tr16_b64_v4i16((__attribute__((address_space(3))) v4s*)p));
}
DI u32x4 ldsread(lds_cptr p) { return *(const __attribute__((address_space(3))) u32x4*)p; }
DI float swapmax(float m) { auto r = __builtin_amdgcn_permlane32_swap(__float_as_uint(m), __float_as_uint(m), false, false); return fmaxf(__uint_as_float(r[0]), __uint_as_float(r[1])); }
DI float max16(const f32x16& s) {
    float a = fmaxf(fmaxf(s[0], s[1]), s[2]), b = fmaxf(fmaxf(s[3], s[4]), s[5]);
    a = fmaxf(fmaxf(a, s[6]), s[7]); b = fmaxf(fmaxf(b, s[8]), s[9]); a = fmaxf(fmaxf(a, s[10]), s[11]); b = fmaxf(fmaxf(b, s[12]), s[13]);
    a = fmaxf(fmaxf(a, s[14]), s[15]); return fmaxf(a, b);
}
DI int crow(int i, int h) { return (i & 3) + 8 * (i >> 2) + 4 * h; }
DI float ex2(float x) { return __builtin_amdgcn_exp2f(x); }

DI unsigned long long mktag(unsigned nonce) { return (unsigned long long)nonce | ((unsigned long long)(~nonce ^ 0x5bd1e995u) << 32); }
DI void publish(char* ws, unsigned line, unsigned long long tag) {
    if ((threadIdx.x & 63) < 8) {
        const u32x4 v = {(unsigned)tag, (unsigned)(tag >> 32), (unsigned)tag, (unsigned)(tag >> 32)};
        bst(v, mkrs(ws + WS_FLAG, 4 * 256 * FS * 8), (threadIdx.x & 63) * 16, line * (FS * 8));
    }
}
template <int SLEEP> DI void wait_flags(const gu64* flags, unsigned idx, bool need, unsigned long long tag) {
#pragma unroll 1
    for (unsigned spins = 0; spins < (1u << 17); ++spins) {
        const unsigned long long x = __hip_atomic_load(flags + idx * FS, RLX_AGENT);
        if (__all(!need || x == tag)) break;
        __builtin_amdgcn_s_sleep(SLEEP);
    }
    asm volatile("" ::: "memory");
}
DI unsigned lds_add(char* lds, int off) {
    unsigned old = 0;
    if ((threadIdx.x & 63) == 0) old = __hip_atomic_fetch_add((__attribute__((address_space(3))) unsigned*)(lds + off), 1u, __ATOMIC_RELAXED, __HIP_MEMORY_SCOPE_WORKGROUP);
    return rfl(old);
}
template <int SLEEP> DI void lds_wait(char* lds, int off, unsigned n) {
    lds_word p = (lds_word)(lds + off);
#pragma unroll 1
    for (unsigned spins = 0; spins < (1u << 20) && *p < n; ++spins) __builtin_amdgcn_s_sleep(SLEEP);
    asm volatile("" ::: "memory");
}

DI f32x16 exact_tile(__amdgpu_buffer_rsrc_t rskv, unsigned kvbase, u32x4 q0, u32x4 q1, int t0, int t1, int tq, bool causal, int lane) {
    const int r32 = lane & 31, h = lane >> 5;
    if (h) q1[2] = 0;
    float m = NEGBIG;
    for (int t = t0; t <= t1; ++t) {
        const u32x4 k0 = bld(rskv, lane * 16, kvbase + t * 4096), k1 = bld(rskv, lane * 16, kvbase + t * 4096 + 1024);
        f32x16 s = {}; s = MFMA(k0, q0, s); s = MFMA(k1, q1, s);
        if (causal) {
#pragma unroll
            for (int i = 0; i < 16; ++i) { const int d = (tq - t) * 32 + r32 - crow(i, h); if (d < 0 || d >= 256) s[i] = NEGBIG; }
        }
        m = fmaxf(m, max16(s));
    }
    m = swapmax(m);
    f32x16 o = {};
    for (int t = t0; t <= t1; ++t) {
        const u32x4 k0 = bld(rskv, lane * 16, kvbase + t * 4096), k1 = bld(rskv, lane * 16, kvbase + t * 4096 + 1024);
        const u32x4 v0 = bld(rskv, lane * 16, kvbase + t * 4096 + 2048), v1 = bld(rskv, lane * 16, kvbase + t * 4096 + 3072);
        f32x16 s = {}; s = MFMA(k0, q0, s); s = MFMA(k1, q1, s);
        u32x4 pa, pb;
#pragma unroll
        for (int i = 0; i < 16; ++i) {
            s[i] = ex2(s[i] - m);
            if (causal) { const int d = (tq - t) * 32 + r32 - crow(i, h); if (d < 0 || d >= 256) s[i] = 0.f; }
        }
#pragma unroll
        for (int i = 0; i < 4; ++i) { pa[i] = cvtpk(s[2 * i], s[2 * i + 1]); pb[i] = cvtpk(s[8 + 2 * i], s[8 + 2 * i + 1]); }
        o = MFMA(v0, pa, o); o = MFMA(v1, pb, o);
    }
    return o;
}
DI float rowsum(const f32x16& o) { auto r = __builtin_amdgcn_permlane32_swap(__float_as_uint(o[12]), __float_as_uint(o[12]), false, false); return __uint_as_float(r[1]); }
DI bool unsafe(const f32x16& o, float l) {
    float z = 0.f;
#pragma unroll
    for (int i = 0; i < 16; ++i) z = __builtin_fmaf(o[i], 0.f, z);
    const bool ok = (z == 0.f) && (l > 0x1p-106f) && (l < 0x1p100f);
    return __any(!ok);
}
DI void store_o(const f32x16& o, float l, __amdgpu_buffer_rsrc_t rso, unsigned off, int lane) {
    const float rl = __builtin_amdgcn_rcpf(l); u32x4 a, c2;
#pragma unroll
    for (int i = 0; i < 4; ++i) { a[i] = cvtpk(o[2 * i] * rl, o[2 * i + 1] * rl); c2[i] = cvtpk(o[8 + 2 * i] * rl, o[8 + 2 * i + 1] * rl); }
    if (lane >> 5) { c2[2] = 0; c2[3] = 0; }
    bst(a, rso, lane * 16, off); bst(c2, rso, lane * 16, off + 1024);
}
#define EXPCVT(S, b4, P, i) do { const float e0_ = ex2(S[b4]), e1_ = ex2(S[b4 + 1]), e2_ = ex2(S[b4 + 2]), e3_ = ex2(S[b4 + 3]); \
        P[i] = cvtpk(e0_, e1_); P[i + 1] = cvtpk(e2_, e3_); PIN(P); } while (0)

struct Proj {
    unsigned ax[4], aw[3];
    f32x16 acc[3];
    u32x4 xf[4], wf[4][3];
    u32x4 id0, id1;
};
DI void p_rdx(Proj& p, lds_cptr L, int buf, int slot, int ks) { p.xf[buf] = ldsread(L + slot * P_SLOT + p.ax[ks]); }
DI void p_rdw(Proj& p, lds_cptr L, int buf, int slot, int ks, int c) {
    lds_cptr wp = L + slot * P_SLOT + p.aw[c] + ks * (16 * P_WP);
    const u32x2 a = trread(wp), b2 = trread(wp + 4 * P_WP);
    p.wf[buf][c] = (u32x4){a[0], a[1], b2[0], b2[1]};
}
template <class Side>
DI void p_kstep(Proj& p, lds_cptr L, int buf, bool first, int nb, bool rd, int slot, int ks2, Side side) {
    const f32x16 zero = {};
    p.acc[0] = MFMA(p.wf[buf][0], p.xf[buf], first ? zero : p.acc[0]); SB(); if (rd) { p_rdx(p, L, nb, slot, ks2); p_rdw(p, L, nb, slot, ks2, 0); } side(0); SB();
    p.acc[1] = MFMA(p.wf[buf][1], p.xf[buf], first ? zero : p.acc[1]); SB(); if (rd) { p_rdw(p, L, nb, slot, ks2, 1); } side(1); SB();
    p.acc[2] = MFMA(p.wf[buf][2], p.xf[buf], first ? zero : p.acc[2]); SB(); if (rd) { p_rdw(p, L, nb, slot, ks2, 2); } side(2); SB();
}
DI void p_setup(Proj& p, int w, int lane, int cb0) {
    const int r32 = lane & 31, h = lane >> 5;
#pragma unroll
    for (int ks = 0; ks < 4; ++ks) p.ax[ks] = (32 * w + r32) * 128 + ((((2 * ks) | h) ^ ((r32 >> 1) & 7)) << 4);
    const int nh = (lane >> 4) & 1, q = (lane & 15) >> 2, pq = lane & 3, pqs = ((pq & 1) << 1) | (pq >> 1);
#pragma unroll
    for (int c = 0; c < 3; ++c) p.aw[c] = P_XB + (8 * h + q) * P_WP + (cb0 & 8) + 56 * c + 32 * nh + 8 * pqs;
#pragma unroll
    for (int e = 0; e < 4; ++e) {
        const int d0 = 8 * h + 2 * e;
        p.id0[e] = (r32 == d0 ? 0x3F80u : 0u) | (r32 == d0 + 1 ? 0x3F800000u : 0u);
        p.id1[e] = (r32 == 16 + d0 ? 0x3F80u : 0u) | (r32 == 17 + d0 ? 0x3F800000u : 0u);
    }
    PIN(p.id0); PIN(p.id1);
#pragma unroll
    for (int i = 0; i < 4; ++i) PIN(p.ax[i]);
#pragma unroll
    for (int i = 0; i < 3; ++i) PIN(p.aw[i]);
}
template <int S0, class Sync, class Piece>
DI void p_run(Proj& p, lds_cptr L, Sync sync, Piece piece) {
    constexpr int sl0 = S0 % P_NS;
    p_rdx(p, L, 0, sl0, 0); p_rdw(p, L, 0, sl0, 0, 0); p_rdw(p, L, 0, sl0, 0, 1); p_rdw(p, L, 0, sl0, 0, 2);
    p_rdx(p, L, 1, sl0, 1); p_rdw(p, L, 1, sl0, 1, 0); p_rdw(p, L, 1, sl0, 1, 1); p_rdw(p, L, 1, sl0, 1, 2); SB();
#pragma unroll
    for (int sb = 0; sb < 7; ++sb) {
        const int s = S0 + sb, sl = s % P_NS, sn = (s + 1) % P_NS;
        p_kstep(p, L, 0, sb == 0, 2, true, sl, 2, [&](int i) { if (sb > 0 && i < 2) piece(s - 1, 5 + i); });
        p_kstep(p, L, 1, false, 3, true, sl, 3, [&](int) {});
        p.acc[0] = MFMA(p.wf[2][0], p.xf[2], p.acc[0]); SB();
        sync(s);
        if (sb < 6) { p_rdx(p, L, 0, sn, 0); p_rdw(p, L, 0, sn, 0, 0); } SB();
        p.acc[1] = MFMA(p.wf[2][1], p.xf[2], p.acc[1]); SB(); if (sb < 6) p_rdw(p, L, 0, sn, 0, 1); piece(s, 0); SB();
        p.acc[2] = MFMA(p.wf[2][2], p.xf[2], p.acc[2]); SB(); if (sb < 6) p_rdw(p, L, 0, sn, 0, 2); piece(s, 1); SB();
        p_kstep(p, L, 3, false, 1, sb < 6, sn, 1, [&](int i) { piece(s, 2 + i); });
    }
}
DI void p_store(Proj& p, char* ws, int b, int jg, int tile, int lane) {
    const int h = lane >> 5;
    const f32x16 zero = {};
    const __amdgpu_buffer_rsrc_t rsall = mkrs(ws, WS_Q_BYTES + WS_KV_BYTES);
    u32x4 f[3][2];
#pragma unroll
    for (int c = 0; c < 3; ++c) {
        const int part = (3 * jg + c) >> 4;
        const f32x16 v = p.acc[c] * (part == 0 ? QSCALE : 1.0f);
#pragma unroll
        for (int i = 0; i < 4; ++i) { f[c][0][i] = cvtpk(v[2 * i], v[2 * i + 1]); f[c][1][i] = cvtpk(v[8 + 2 * i], v[8 + 2 * i + 1]); }
        if (h) { f[c][1][2] = part == 0 ? 0u : 0x00003F80u; f[c][1][3] = 0; }
        if (part == 2) { p.acc[c] = MFMA(f[c][0], p.id0, zero); p.acc[c] = MFMA(f[c][1], p.id1, p.acc[c]); }
        else {
            const unsigned th = (b * NH + ((3 * jg + c) & 15)) * NT + tile, o = part == 0 ? WS_Q + th * 2048u : WS_KV + th * 4096u;
            bst(f[c][0], rsall, lane * 16, o); bst(f[c][1], rsall, lane * 16 + 1024, o);
        }
    }
#pragma unroll
    for (int c = 0; c < 3; ++c) {
        if (((3 * jg + c) >> 4) == 2) {
#pragma unroll
            for (int i = 0; i < 4; ++i) { f[c][0][i] = cvtpk(p.acc[c][2 * i], p.acc[c][2 * i + 1]); f[c][1][i] = cvtpk(p.acc[c][8 + 2 * i], p.acc[c][8 + 2 * i + 1]); }
            const unsigned o = WS_KV + ((b * NH + ((3 * jg + c) & 15)) * NT + tile) * 4096u + 2048u;
            bst(f[c][0], rsall, lane * 16, o); bst(f[c][1], rsall, lane * 16 + 1024, o);
        }
    }
}

DI void out_tile(char* lds, void* out, __amdgpu_buffer_rsrc_t rso, int ob, int otb, int og, int w, int lane) {
    const int r32 = lane & 31, h = lane >> 5;
    const unsigned lo = lane * 16;
    lds_cptr L = (lds_cptr)lds;
    const f32x16 zero = {};
    const int tile = otb * 4 + w;
    const unsigned obase = (ob * NH * NT + tile) * 2048u;
    u32x4 of[32];
#pragma unroll
    for (int i = 0; i < 32; ++i) of[i] = bld(rso, lo + (i & 1) * 1024, obase + (i >> 1) * (NT * 2048));
    unsigned aw;
    {
        const int nh = (lane >> 4) & 1, q = (lane & 15) >> 2, pq = lane & 3, pqs = ((pq & 1) << 1) | (pq >> 1);
        const int cb = 32 * nh + 8 * pqs;
        aw = LDS_WO + (4 * h + q) * 128 + (((cb >> 4) ^ (4 * (q >> 1))) << 4) + (cb & 8);
    }
    lds_wait<0>(lds, C_WO, 4);
    f32x16 acc[2];
    u32x4 wf[2][2];
    auto rd = [&](int buf, int ks, int fb) {
        const int hd = ks >> 1, s2 = ks & 1;
        lds_cptr wp = L + ((aw ^ (fb * 64)) + (28 * hd + 16 * s2) * 128);
        const u32x2 x = trread(wp), y = trread(wp + 8 * 128);
        wf[buf][fb] = (u32x4){x[0], x[1], y[0], y[1]};
    };
    rd(0, 0, 0); rd(0, 0, 1); SB();
#pragma unroll
    for (int ks = 0; ks < 32; ++ks) {
        acc[0] = MFMA(wf[ks & 1][0], of[ks], ks == 0 ? zero : acc[0]); SB(); if (ks + 1 < 32) rd((ks + 1) & 1, ks + 1, 0); SB();
        acc[1] = MFMA(wf[ks & 1][1], of[ks], ks == 0 ? zero : acc[1]); SB(); if (ks + 1 < 32) rd((ks + 1) & 1, ks + 1, 1); SB();
    }
    const __amdgpu_buffer_rsrc_t rsout = mkrs(out, SEQ * 2 * DM * 2);
    char* stg = lds + LDS_OST + w * 4096;
#pragma unroll
    for (int fb = 0; fb < 2; ++fb) {
        u32x4 x, y;
#pragma unroll
        for (int i = 0; i < 4; ++i) { x[i] = cvtpk(acc[fb][2 * i], acc[fb][2 * i + 1]); y[i] = cvtpk(acc[fb][8 + 2 * i], acc[fb][8 + 2 * i + 1]); }
        *(u32x4*)(stg + r32 * 128 + (((fb * 4 + h) ^ (r32 & 7)) << 4)) = x;
        *(u32x4*)(stg + r32 * 128 + (((fb * 4 + 2 + h) ^ (r32 & 7)) << 4)) = y;
    }
    const int a = lane >> 3, pc = lane & 7;
#pragma unroll
    for (int k = 0; k < 4; ++k) {
        const u32x4 v = *(const u32x4*)(stg + (8 * k + a) * 128 + ((pc ^ a) << 4));
        bst(v, rsout, (tile * 32 + 8 * k + a) * (2 * DM * 2) + pc * 16, ob * (DM * 2) + og * 128);
    }
}

__global__ __launch_bounds__(512, 2) void k_main(const void* __restrict__ xA, const void* __restrict__ wA, const void* __restrict__ woA, const void* __restrict__ xB,
                                                 const void* __restrict__ wB, const void* __restrict__ woB, char* __restrict__ ws, void* __restrict__ out) {
    unsigned long long kpc; asm volatile("s_getpc_b64 %0" : "=s"(kpc)); kpc &= ~255ull;
    extern __shared__ __attribute__((aligned(16))) char lds[];
    const int tid = threadIdx.x, lane = tid & 63, wv = rfl(tid >> 6), w = wv & 3;
    const int id = blockIdx.x;
    const int pc_ = id & 7, pr = id >> 3;
    const int tb = (pc_ & 3) * 4 + (pr & 3);
    const int jg = (int)(((pc_ < 4 ? 0xBA54C963u : 0xFED87210u) >> (4 * (pr >> 2))) & 15);
    const int head = 2 * (id & 7) + (id >> 7), j = (id >> 3) & 15;
    const int tq = 4 * j + w;
    const int ob_ = pc_ >> 2, otb = (pc_ & 3) * 4 + (pr & 3), og = pr >> 2;

    gu64* flags = (gu64*)(ws + WS_FLAG);
    gu64* oflags = (gu64*)(ws + WS_OFLAG);
    const unsigned lds0 = (unsigned)(uintptr_t)lds;
    const int r32 = lane & 31, h = lane >> 5;
    const unsigned lo = lane * 16;
    const __amdgpu_buffer_rsrc_t rsq = mkrs(ws + WS_Q, WS_Q_BYTES), rskv = mkrs(ws + WS_KV, WS_KV_BYTES);
    const int cb0 = 168 * jg;
    const int gk = (16 + head) / 3, gv = (32 + head) / 3, gq = head / 3;
    const f32x16 zero = {};
    lds_cptr L = (lds_cptr)lds;

    const __amdgpu_buffer_rsrc_t rsxB = mkrs(xB, SEQ * DM * 2), rswB = mkrs(wB, DM * NQKV * 2), rsxA = mkrs(xA, SEQ * DM * 2), rswA = mkrs(wA, DM * NQKV * 2);
    unsigned vx[4] = {}, vw[3] = {};
    auto stage = [&](int s) {
        const unsigned slot = lds0 + (s % P_NS) * P_SLOT;
        const int sb = s >= 7 ? s - 7 : s;
        const __amdgpu_buffer_rsrc_t rx = s >= 7 ? rsxA : rsxB, rw = s >= 7 ? rswA : rswB;
#pragma unroll
        for (int i = 0; i < 4; ++i) dma16(rx, vx[i], rfl(sb * 128), rfl(slot + (4 * w + i) * 1024));
#pragma unroll
        for (int i = 0; i < 3; ++i) dma16(rw, vw[i], rfl(sb * 64 * NQKV * 2), rfl(slot + P_XB + (3 * w + i) * 1024));
    };
    if (wv >= 4) {
        const int a = lane >> 3, pc = lane & 7;
#pragma unroll
        for (int i = 0; i < 4; ++i) { const int row = tb * 128 + 32 * w + 8 * i + a; vx[i] = row * (DM * 2) + ((pc ^ (((a >> 1) | (4 * (i & 1))))) << 4); }
#pragma unroll
        for (int i = 0; i < 3; ++i) {
            const int u = 64 * (3 * w + i) + lane, row = u / 12, pp = u - 12 * row;
            vw[i] = row * (NQKV * 2) + (cb0 & ~15) + (pp > 10 ? 10 : pp) * 16;
        }
        stage(0); stage(1);
    }
    int role = wv; asm volatile("" : "+s"(role));

    if (role >= 4) {
        Proj p; p_setup(p, w, lane, cb0);
        auto piece1 = [&](int s, int n) {
            const unsigned slot = lds0 + (s % P_NS) * P_SLOT;
            const int sb = s - 7;
            if (n < 4) dma16(rsxA, vx[n], rfl(sb * 128), rfl(slot + (4 * w + n) * 1024));
            else dma16(rswA, vw[n - 4], rfl(sb * 64 * NQKV * 2), rfl(slot + P_XB + (3 * w + n - 4) * 1024));
        };
#define L_WAIT(n) asm volatile("s_waitcnt vmcnt(" #n ")\n\ts_barrier" ::: "memory")
        L_WAIT(7);
        stage(2); L_WAIT(7);
        stage(3); L_WAIT(7);
        stage(4); L_WAIT(7);
        stage(5); L_WAIT(7);
        stage(6); L_WAIT(7);
        stage(7); L_WAIT(7);
        stage(8); stage(9);
        __builtin_amdgcn_s_setprio(1);
        unsigned ybar = 0;
        auto ymeet = [&]() { asm volatile("s_waitcnt lgkmcnt(0)" ::: "memory"); lds_add(lds, C_YBAR); ybar += 4; lds_wait<0>(lds, C_YBAR, ybar); };
        lds_wait<1>(lds, C_XIS, 4);
        stage(10); stage(11);
        asm volatile("s_waitcnt vmcnt(14)" ::: "memory"); ymeet();
        p_run<7>(p, L, [&](int s) {
            if (s == 8 || s == 11) { asm volatile("s_waitcnt vmcnt(0)" ::: "memory"); ymeet(); }
        }, [&](int s, int n) {
            if (s == 8) { piece1(12 + (2 * n) / 7, (2 * n) % 7); piece1(12 + (2 * n + 1) / 7, (2 * n + 1) % 7); }
        });
        p_store(p, ws, 0, jg, tb * 4 + w, lane);
        const unsigned long long tag = mktag(*(lds_word)(lds + C_NONCE) + 1u);
        asm volatile("s_waitcnt vmcnt(0)" ::: "memory");
        if (lds_add(lds, C_YST) == 3) publish(ws, jg * 16 + tb, tag);
        if (pr < 28) {
            lds_wait<1>(lds, C_YST, 4);
            const __amdgpu_buffer_rsrc_t rsw = mkrs(ob_ ? woB : woA, DM * DM * 2);
            const int a = lane >> 3, pc = lane & 7;
            const unsigned v = a * (DM * 2) + og * 128 + ((pc ^ (4 * ((a >> 1) & 1))) << 4);
#pragma unroll 1
            for (int i = w; i < 57; i += 4) dma16(rsw, v, rfl((i < 56 ? i : 55) * 8 * DM * 2), rfl(lds0 + LDS_WO + i * 1024));
            asm volatile("s_waitcnt vmcnt(0)" ::: "memory");
            lds_add(lds, C_WO);
        }
        if (w == 0) {
            const int a3 = lane % 3, blk = j - a3;
            const unsigned idx = lane < 3 ? gk * 16 + blk : lane < 6 ? gv * 16 + blk : gq * 16 + j;
            wait_flags<8>(flags, (blk < 0 && lane < 6) ? 0 : idx, lane <= 6 && !(blk < 0 && lane < 6), tag);
            if (lane == 0) *(lds_word)(lds + C_AOK) = 1;
        } else {
            lds_wait<8>(lds, C_AOK, 1);
        }
        __builtin_amdgcn_s_setprio(0);
        const unsigned kvA = head * NT * 4096u, qoA = (head * NT + tq) * 2048u;
        const u32x4 qa0 = bld(rsq, lo, qoA); u32x4 qa1 = bld(rsq, lo + 1024, qoA);
        f32x16 oa = {};
        {
            u32x4 kb[9][2], vb[9][2];
#pragma unroll
            for (int u = 0; u < 9; ++u) {
                const unsigned so = (tq - 8 + u >= 0) ? kvA + (tq - 8 + u) * 4096u : WS_KV_BYTES;
                kb[u][0] = bld(rskv, lo, so); kb[u][1] = bld(rskv, lo + 1024, so); vb[u][0] = bld(rskv, lo + 2048, so); vb[u][1] = bld(rskv, lo + 3072, so);
            }
            f32x16 cd, cm;
#pragma unroll
            for (int i = 0; i < 16; ++i) { cd[i] = (crow(i, h) > r32) ? NEGBIG : 0.f; cm[i] = __uint_as_float(__float_as_uint(cd[i]) ^ __float_as_uint(NEGBIG)); }
            {
                f32x16 s = MFMA(kb[8][0], qa0, cd); s = MFMA(kb[8][1], qa1, s);
                const float m = swapmax(max16(s));
                if (h) qa1[2] = cvtpk(-(m + DELTA), 0.f);
            }
            f32x16 sA, sB; u32x4 pa, pb;
            sA = MFMA(kb[0][0], qa0, cm); sA = MFMA(kb[0][1], qa1, sA);
#define AUNIT(SC, SN, U)                                                                                                  \
            { if ((U) < 8) { SN = MFMA(kb[((U) + 1) % 9][0], qa0, ((U) == 7 ? cd : zero)); } SB();                         \
              EXPCVT(SC, 0, pa, 0); SB();                                                                                 \
              if ((U) < 8) { SN = MFMA(kb[((U) + 1) % 9][1], qa1, SN); } SB();                                             \
              EXPCVT(SC, 4, pa, 2); SB();                                                                                 \
              if ((U) > 0) { oa = MFMA(vb[((U) + 8) % 9][1], pb, oa); } SB();                                              \
              EXPCVT(SC, 8, pb, 0); SB();                                                                                 \
              oa = MFMA(vb[U][0], pa, oa); SB();                                                                          \
              EXPCVT(SC, 12, pb, 2); SB(); }
            __builtin_amdgcn_s_setprio(1);
            AUNIT(sA, sB, 0) AUNIT(sB, sA, 1)
            __builtin_amdgcn_s_setprio(0);
            AUNIT(sA, sB, 2) AUNIT(sB, sA, 3) AUNIT(sA, sB, 4) AUNIT(sB, sA, 5) AUNIT(sA, sB, 6) AUNIT(sB, sA, 7) AUNIT(sA, sB, 8)
            oa = MFMA(vb[8][1], pb, oa);
        }
        float la = rowsum(oa);
        if (unsafe(oa, la)) { oa = exact_tile(rskv, kvA, qa0, qa1, max(tq - 8, 0), tq, tq, true, lane); la = rowsum(oa); }
        store_o(oa, la, rsq, qoA, lane);
        asm volatile("s_waitcnt vmcnt(0)" ::: "memory");
        if (lds_add(lds, C_YOS) == 3) publish(ws, 512 + j * 16 + head, tag);
        if (pr >= 28 || ob_ != 0) __builtin_amdgcn_endpgm();
        if (w == 0) {
            wait_flags<4>(oflags + otb * 16 * FS, lane & 15, true, tag);
            if (lane == 0) *(lds_word)(lds + C_OOK) = 1;
        } else lds_wait<4>(lds, C_OOK, 1);
        out_tile(lds, out, rsq, 0, otb, og, w, lane);
        __builtin_amdgcn_endpgm();
    }

    if (tid < N_CTL) *(lds_word)(lds + LDS_CTL + 16 * tid) = 0;
    unsigned t_in0, t_in1, t_misc, t_code, nonce;
    {
        const int gw = id * 4 + w;
        if (gw >= 371) __builtin_amdgcn_s_sleep(24);
        const char* base; int c = gw < 840 ? gw : 0;
        if (c < 224) base = (const char*)xB; else if ((c -= 224) < 147) base = (const char*)wB; else if ((c -= 147) < 224) base = (const char*)xA;
        else if ((c -= 224) < 147) base = (const char*)wA; else if ((c -= 147) < 49) base = (const char*)woA; else { c -= 49; base = (const char*)woB; }
        const char* a0 = base + c * 8192 + lane * 64; const char* a1 = a0 + 4096;
        TOUCH(t_in0, a0); TOUCH(t_in1, a1);
        const int l4 = lane < 4 ? lane : 0, l42 = lane < 42 ? lane : 0;
        const char* a2 = w == 1 ? ws + WS_FLAG + (id * 4 + l4) * (FS * 8)
                       : w != 2 ? (const char*)xB
                       : l42 < 19 ? (const char*)wB + l42 * 65536 : l42 < 38 ? (const char*)wA + (l42 - 19) * 65536
                       : (l42 < 40 ? (const char*)xB : (const char*)xA) + tb * (128 * DM * 2) + (l42 & 1) * 65536;
        asm volatile("global_load_dword %0, %1, off sc1" : "=&v"(t_misc) : "v"(a2) : "memory");
        const char* a3 = ws + WS_NONCE;
        asm volatile("global_load_dword %0, %1, off sc1" : "=&v"(nonce) : "v"(a3) : "memory");
        const unsigned off = (pr * 4 + w) * 256 + (lane & 3) * 64;
        const char* a4 = (const char*)kpc + (off < CODE_TOUCH ? off : 0);
        TOUCH(t_code, a4);
    }
    {
        Proj p; p_setup(p, w, lane, cb0);
#define P_BAR() asm volatile("s_waitcnt lgkmcnt(0)\n\ts_barrier" ::: "memory")
        P_BAR();
        p_run<0>(p, L, [&](int s) {
            if (s == 5) {
                asm volatile("s_waitcnt vmcnt(0)" : "+v"(t_in0), "+v"(t_in1), "+v"(t_misc), "+v"(t_code), "+v"(nonce) :: "memory");
                if (tid == 0) *(lds_word)(lds + C_NONCE) = nonce;
            }
            if (s < 6) P_BAR();
        }, [&](int, int) {});
        p_store(p, ws, 1, jg, tb * 4 + w, lane);
    }
    lds_add(lds, C_XIS);
    nonce += 1u;
    const unsigned long long tag = mktag(nonce);
    asm volatile("s_waitcnt vmcnt(0)" ::: "memory");
    if (lds_add(lds, C_XST) == 3) publish(ws, 256 + jg * 16 + tb, tag);

    const unsigned kvB = (NH + head) * NT * 4096u, kvw = kvB + 16 * w * 4096u;
    u32x4 q[4][2], kb[4][2], vb[4][2];
    const gu64* fB = flags + 256 * FS + (lane < 8 ? (lane < 4 ? gk : gv) * 16 + 4 * w + (lane & 3) : lane == 8 ? gq * 16 + j : gk * 16 + j) * FS;
    unsigned long long seen = 0;
#pragma unroll 1
    for (unsigned spins = 0; spins < (1u << 17); ++spins) {
        seen = __ballot(__hip_atomic_load(fB, RLX_AGENT) == tag);
        if ((seen & 0x300) == 0x300) break;
        __builtin_amdgcn_s_sleep(4);
    }
    asm volatile("" ::: "memory");
    const unsigned qoB = ((NH + head) * NT + tq) * 2048u;
    u32x4 q0 = bld(rsq, lo, qoB), q1 = bld(rsq, lo + 1024, qoB);
    const u32x4 kd0 = bld(rskv, lo, kvB + tq * 4096u), kd1 = bld(rskv, lo + 1024, kvB + tq * 4096u);
    SB();
#pragma unroll 1
    for (unsigned spins = 0; spins < (1u << 17) && (seen & 0x11) != 0x11; ++spins) {
        seen = __ballot(__hip_atomic_load(fB, RLX_AGENT) == tag);
        if ((seen & 0x11) != 0x11) __builtin_amdgcn_s_sleep(4);
    }
    asm volatile("" ::: "memory");
    kb[0][0] = bld(rskv, lo, kvw); kb[0][1] = bld(rskv, lo + 1024, kvw); vb[0][0] = bld(rskv, lo + 2048, kvw); vb[0][1] = bld(rskv, lo + 3072, kvw);
    unsigned long long x2 = tag;
    if ((seen & 0xFF) != 0xFF) x2 = __hip_atomic_load(fB, RLX_AGENT);
    SB();
    {
        f32x16 s = MFMA(kd0, q0, zero); s = MFMA(kd1, q1, s);
        const float m = swapmax(max16(s));
        if (h) q1[2] = cvtpk(-(m + DELTA), 0.f);
    }
    *(u32x4*)(lds + LDS_QX + w * 2048 + lo) = q0; *(u32x4*)(lds + LDS_QX + w * 2048 + 1024 + lo) = q1;
    asm volatile("s_waitcnt lgkmcnt(0)" ::: "memory");
    lds_add(lds, C_QX);
#pragma unroll
    for (int t = 1; t < 3; ++t) { kb[t][0] = bld(rskv, lo, kvw + t * 4096); kb[t][1] = bld(rskv, lo + 1024, kvw + t * 4096); vb[t][0] = bld(rskv, lo + 2048, kvw + t * 4096); vb[t][1] = bld(rskv, lo + 3072, kvw + t * 4096); }
    SB();
    lds_wait<0>(lds, C_QX, 4);
#pragma unroll
    for (int i = 0; i < 4; ++i) { const int qb = (w + 1 + i) & 3;
        q[i][0] = *(const u32x4*)(lds + LDS_QX + qb * 2048 + lo); q[i][1] = *(const u32x4*)(lds + LDS_QX + qb * 2048 + 1024 + lo); }
    if ((__ballot(x2 == tag) & 0xFF) != 0xFF) wait_flags<4>(fB, 0, lane < 8, tag);

    vb[3][1] = (u32x4){0, 0, 0, 0};
    f32x16 o[4] = {}; f32x16 sA, sB;
    u32x4 pa, pb = {0, 0, 0, 0};
#pragma unroll
    for (int i = 0; i < 4; ++i) { PIN(q[i][0]); PIN(q[i][1]); }
    sA = MFMA(kb[0][0], q[0][0], zero); sA = MFMA(kb[0][1], q[0][1], sA);
#define UNIT(SC, SN, T, B, LD)                                                                                           \
    { constexpr int Tn = ((B) == 3) ? (((T) + 1) & 3) : (T), Bn = ((B) + 1) & 3, Tp = ((B) == 0) ? (((T) + 3) & 3) : (T), Bp = ((B) + 3) & 3; \
      if ((B) == 3) { asm volatile("" :: "v"(kb[Tn][0]), "v"(kb[Tn][1])); SB(); }        \
      SN = MFMA(kb[Tn][0], q[Bn][0], zero); SB(); EXPCVT(SC, 0, pa, 0); SB();                                         \
      SN = MFMA(kb[Tn][1], q[Bn][1], SN); SB(); EXPCVT(SC, 4, pa, 2); SB();                                           \
      o[Bp] = MFMA(vb[Tp][1], pb, o[Bp]); SB(); LD; SB(); EXPCVT(SC, 8, pb, 0); SB();                                 \
      if ((B) == 0) { asm volatile("" :: "v"(vb[T][0]), "v"(vb[T][1])); SB(); }                \
      o[B] = MFMA(vb[T][0], pa, o[B]); SB(); EXPCVT(SC, 12, pb, 2); SB(); }
#define TILE(T, so)                                                                                                      \
    UNIT(sA, sB, T, 0, (kb[((T) + 3) & 3][0] = bld(rskv, lo, so)))                                                    \
    UNIT(sB, sA, T, 1, (kb[((T) + 3) & 3][1] = bld(rskv, lo + 1024, so)))                                             \
    UNIT(sA, sB, T, 2, (vb[((T) + 3) & 3][0] = bld(rskv, lo + 2048, so)))                                             \
    UNIT(sB, sA, T, 3, (vb[((T) + 3) & 3][1] = bld(rskv, lo + 3072, so)))
    auto put = [&](int i) {
        char* dst = lds + LDS_PART + (((w + 1 + i) & 3) * 3 + i) * 4096 + lo;
#pragma unroll
        for (int g = 0; g < 4; ++g) *(float4*)(dst + g * 1024) = make_float4(o[i][4 * g], o[i][4 * g + 1], o[i][4 * g + 2], o[i][4 * g + 3]);
    };
    unsigned sn = kvw + 3 * 4096u;
#pragma unroll 1
    for (int t = 0; t < 16; t += 4) {
        const unsigned sl = kvw + 15 * 4096u;
        const unsigned s0 = sn, s1 = min(sn + 4096u, sl), s2 = min(sn + 8192u, sl), s3 = min(sn + 12288u, sl); sn += 16384u;
        if (t == 12) lds_wait<2>(lds, C_YST, 4);
        TILE(0, s0) TILE(1, s1) TILE(2, s2)
        UNIT(sA, sB, 3, 0, (kb[2][0] = bld(rskv, lo, s3)))
        UNIT(sB, sA, 3, 1, (kb[2][1] = bld(rskv, lo + 1024, s3)))
        UNIT(sA, sB, 3, 2, (vb[2][0] = bld(rskv, lo + 2048, s3)))
        if (t == 12) put(0);
        SB();
        UNIT(sB, sA, 3, 3, (vb[2][1] = bld(rskv, lo + 3072, s3)))
        if (t == 12) put(1);
    }
    o[3] = MFMA(vb[3][1], pb, o[3]);
    put(2);
    f32x16 ob = o[3];
    const u32x4 qb0 = q[3][0], qb1 = q[3][1];
    asm volatile("s_waitcnt lgkmcnt(0)" ::: "memory");
    lds_add(lds, C_PART); lds_wait<0>(lds, C_PART, 4);
#pragma unroll
    for (int sl = 0; sl < 3; ++sl) {
        const char* src = lds + LDS_PART + (w * 3 + sl) * 4096 + lo;
#pragma unroll
        for (int g = 0; g < 4; ++g) { const float4 x = *(const float4*)(src + g * 1024); ob[4 * g] += x.x; ob[4 * g + 1] += x.y; ob[4 * g + 2] += x.z; ob[4 * g + 3] += x.w; }
    }
    float lb = rowsum(ob);
    if (unsafe(ob, lb)) { ob = exact_tile(rskv, kvB, qb0, qb1, 0, NT - 1, tq, false, lane); lb = rowsum(ob); }
    store_o(ob, lb, rsq, qoB, lane);
    asm volatile("s_waitcnt vmcnt(0)" ::: "memory");
    if (lds_add(lds, C_XOS) == 3) publish(ws, 768 + j * 16 + head, tag);

    if (pr < 28 && ob_ == 1) {
        if (w == 0) {
            wait_flags<4>(oflags + (256 + otb * 16) * FS, lane & 15, true, tag);
            if (lane == 0) *(lds_word)(lds + C_OOK) = 1;
        } else lds_wait<4>(lds, C_OOK, 1);
        out_tile(lds, out, rsq, 1, otb, og, w, lane);
    }
    if (id == 0 && w == 0) {
#pragma unroll
        for (int i = 0; i < 4; ++i) wait_flags<4>(flags + 256 * FS, i * 64 + lane, true, tag);
        if (lane == 0) __hip_atomic_store((gu32*)(ws + WS_NONCE), nonce, RLX_AGENT);
    }
    __builtin_amdgcn_s_waitcnt(0);
    __builtin_amdgcn_endpgm();
}

extern "C" void kernel_launch(void* const* d_in, const int* in_sizes, int n_in, void* d_out, int out_size, void* d_ws, size_t ws_size, hipStream_t stream) {
    (void)hipFuncSetAttribute((const void*)k_main, hipFuncAttributeMaxDynamicSharedMemorySize, LDS_BYTES);
    k_main<<<256, 512, LDS_BYTES, stream>>>(d_in[0], d_in[1], d_in[2], d_in[3], d_in[4], d_in[5], (char*)d_ws, d_out);
}
```
